```python
import torch

def _softmax(x, axis=-1):
    return torch.softmax(x, dim=axis)
def _gelu(x):
    return torch.nn.functional.gelu(x, approximate="tanh")
def _silu(x):
    return torch.nn.functional.silu(x)
def _sigmoid(x):
    return torch.sigmoid(x)
def _layernorm(x, axis=-1, eps=1e-5):
    m = x.mean(dim=axis, keepdim=True)
    v = x.var(dim=axis, keepdim=True, unbiased=False)
    return (x - m) / torch.sqrt(v + eps)
def _scatter_add(tgt, idx, src, axis=0):
    return tgt.clone().index_add_(axis, idx, src)
def _segment_sum(src, seg, n):
    out = torch.zeros((n,) + src.shape[1:], dtype=src.dtype)
    return out.index_add_(0, seg, src)
def _pad(x, pw):
    flat = tuple(v for pair in reversed(pw) for v in pair)
    return torch.nn.functional.pad(x, flat)

kernel_name = 'sequential_preLN__mha_block_cached__glu_gated_glu__decode'

def setup_inputs():

    _w = (lambda a: (lambda t: t.to(torch.bfloat16) if t.is_floating_point() else t)(torch.as_tensor(a)))
    def _A_setup():
        B, Sq, D = 1, 1, 640
        H, Sk, Dh = 32, 32768, 192
        return {
            "x": _w((lambda *s: torch.randn(*s, dtype=torch.bfloat16) * 0.05)(B, Sq, D)),
            "k_cache": _w((lambda *s: torch.randn(*s, dtype=torch.bfloat16) * 3.2)(B, H, Sk, Dh)),
            "v_cache": _w((lambda *s: torch.randn(*s, dtype=torch.bfloat16) * 0.4)(B, H, Sk, Dh)),
            "wq": _w((lambda *s: torch.randn(*s, dtype=torch.bfloat16) * 3.2)(D, H * Dh) * (D ** -0.5)),
            "wo": _w((lambda *s: torch.randn(*s, dtype=torch.bfloat16) * 0.4)(H * Dh, D) * ((H * Dh) ** -0.5)),
        }
    _A_in = _A_setup()

    def _B_setup():
        B, S, D, F = 1, 1, 640, 1536
        return {
            "x": _w((lambda *s: torch.randn(*s, dtype=torch.bfloat16) * 0.05)(B, S, D)),
            "w_gate": _w((lambda *s: torch.randn(*s, dtype=torch.bfloat16) * 0.1)(D, F)),
            "w_up": _w((lambda *s: torch.randn(*s, dtype=torch.bfloat16) * 0.05)(D, F)),
            "w_down": _w((lambda *s: torch.randn(*s, dtype=torch.bfloat16) * 0.05)(F, D)),
        }
    _B_in = _B_setup()

    return {
        "A_x": _A_in["x"],
        "A_k_cache": _A_in["k_cache"],
        "A_v_cache": _A_in["v_cache"],
        "A_wq": _A_in["wq"],
        "A_wo": _A_in["wo"],
        "B_x": _B_in["x"],
        "B_w_gate": _B_in["w_gate"],
        "B_w_up": _B_in["w_up"],
        "B_w_down": _B_in["w_down"],
    }

def reference(A_x, A_k_cache, A_v_cache, A_wq, A_wo, B_x, B_w_gate, B_w_up, B_w_down):
    def _A_ref(x, k_cache, v_cache, wq, wo):
        B, Sq, D = x.shape
        H, Dh = 32, 192
        q = (lambda x, *d: x.permute(*d))((x @ wq).reshape(B, Sq, H, Dh), 0, 2, 1, 3)
        scores = torch.einsum("bhqd,bhkd->bhqk", q, k_cache) * (Dh ** -0.5)
        p = _softmax(scores, axis=-1)
        o = torch.einsum("bhqk,bhkd->bhqd", p, v_cache)
        return (lambda x, *d: x.permute(*d))(o, 0, 2, 1, 3).reshape(B, Sq, H * Dh) @ wo

    def _B_ref(x, w_gate, w_up, w_down):
        g = torch.einsum("bsd,df->bsf", x, w_gate)
        u = torch.einsum("bsd,df->bsf", x, w_up)
        h = _sigmoid(g) * u
        return torch.einsum("bsf,fd->bsd", h, w_down)

    x = A_x
    h = x + _A_ref(_layernorm(x, axis=-1), A_k_cache, A_v_cache, A_wq, A_wo)
    h2 = h + _B_ref(_layernorm(h, axis=-1), B_w_gate, B_w_up, B_w_down)
    return h2
```

```cpp
#include <hip/hip_runtime.h>
#include <hip/hip_bf16.h>
#include <stdint.h>

#define MIMIC 0

constexpr int D = 640, H = 32, SK = 32768, DH = 192, F = 1536, HD = H * DH;
constexpr int NSPLIT = 16;
constexpr int KPS = SK / NSPLIT;

typedef unsigned u32x4 __attribute__((ext_vector_type(4)));
typedef float f32x4 __attribute__((ext_vector_type(4)));
typedef __bf16 bf16x2 __attribute__((ext_vector_type(2)));

__device__ __forceinline__ float bflo(unsigned u) { return __builtin_bit_cast(float, u << 16); }
__device__ __forceinline__ float bfhi(unsigned u) { return __builtin_bit_cast(float, u & 0xffff0000u); }
__device__ __forceinline__ float bf2f(unsigned short u) { return __builtin_bit_cast(float, ((unsigned)u) << 16); }
__device__ __forceinline__ float rbf(float x) {
  __bf16 b = (__bf16)x; return (float)b;
}
__device__ __forceinline__ float rb(float x) {
  return x;
}
__device__ __forceinline__ float dot2(unsigned k, unsigned q, float acc) {
  return __builtin_amdgcn_fdot2_f32_bf16(__builtin_bit_cast(bf16x2, k), __builtin_bit_cast(bf16x2, q), acc, false);
}
template <int CTRL> __device__ __forceinline__ float dpp(float x) {
  return __builtin_bit_cast(float, __builtin_amdgcn_mov_dpp(__builtin_bit_cast(int, x), CTRL, 0xf, 0xf, true));
}
constexpr int XOR1 = 0xB1, XOR2 = 0x4E, XOR7 = 0x141, XOR8 = 0x128;

__device__ __forceinline__ float wave_sum(float v) {
  for (int o = 32; o > 0; o >>= 1) v += __shfl_xor(v, o);
  return v;
}

__device__ __forceinline__ void ln640(const float* in, float* out, float* red, int tid, int nthr) {
  float s = 0.f;
  for (int i = tid; i < D; i += nthr) s += in[i];
  s = wave_sum(s);
  if ((tid & 63) == 0) red[tid >> 6] = s;
  __syncthreads();
  float tot = 0.f;
  for (int w = 0; w < nthr / 64; ++w) tot += red[w];
  const float mean = tot / D;
  __syncthreads();
  float v = 0.f;
  for (int i = tid; i < D; i += nthr) { float d = in[i] - mean; v += d * d; }
  v = wave_sum(v);
  if ((tid & 63) == 0) red[tid >> 6] = v;
  __syncthreads();
  float vt = 0.f;
  for (int w = 0; w < nthr / 64; ++w) vt += red[w];
  const float var = rb(vt / D);
  const float m = rb(mean);
  const float den = rb(sqrtf(rb(var + 1e-5f)));
  for (int i = tid; i < D; i += nthr) out[i] = rb(rb(in[i] - m) / den);
  __syncthreads();
}

__global__ void __launch_bounds__(256) k_qproj(const unsigned short* x, const unsigned short* wq, float* q) {
  __shared__ float xs[D], xn[D], red[4], part[32][64];
  const int tid = threadIdx.x;
  for (int i = tid; i < D; i += 256) xs[i] = bf2f(x[i]);
  __syncthreads();
  ln640(xs, xn, red, tid, 256);
  const int cg = tid & 7, rg = tid >> 3, c0 = blockIdx.x * 64 + cg * 8;
  float acc[8] = {};
#pragma unroll 4
  for (int r = rg; r < D; r += 32) {
    u32x4 w = *(const u32x4*)(wq + (size_t)r * HD + c0);
    float xv = xn[r];
    for (int k = 0; k < 4; ++k) { acc[2 * k] += xv * bflo(w[k]); acc[2 * k + 1] += xv * bfhi(w[k]); }
  }
  for (int k = 0; k < 8; ++k) part[rg][cg * 8 + k] = acc[k];
  __syncthreads();
  if (tid < 64) {
    float s = 0.f;
    for (int g = 0; g < 32; ++g) s += part[g][tid];
    q[blockIdx.x * 64 + tid] = s;
  }
}

constexpr int U = 4;
__global__ void __launch_bounds__(256) k_attn(const unsigned short* __restrict__ kc, const unsigned short* __restrict__ vc,
                                              const float* __restrict__ q, float* __restrict__ pml, float* __restrict__ po) {
  __shared__ float mrg[4][8][2];
  __shared__ float macc[4][DH];
  const int s = blockIdx.x, h = blockIdx.y;
  const int tid = threadIdx.x, lane = tid & 63, wave = __builtin_amdgcn_readfirstlane(tid >> 6);
  const int r = lane >> 3, seg = lane & 7;
  unsigned qp[12];
  unsigned ql[12];
#pragma unroll
  for (int i = 0; i < 3; ++i)
#pragma unroll
    for (int k = 0; k < 4; ++k) {
      float a = q[h * DH + i * 64 + seg * 8 + 2 * k], b = q[h * DH + i * 64 + seg * 8 + 2 * k + 1];
      float ah = rbf(a), bh = rbf(b);
      qp[i * 4 + k] = (__builtin_bit_cast(unsigned, ah) >> 16) | (__builtin_bit_cast(unsigned, bh) & 0xffff0000u);
      float al = rbf(a - ah), bl = rbf(b - bh);
      ql[i * 4 + k] = (__builtin_bit_cast(unsigned, al) >> 16) | (__builtin_bit_cast(unsigned, bl) & 0xffff0000u);
    }
  const size_t base = ((size_t)h * SK + (size_t)s * KPS) * DH;
  const int nb = KPS / (8 * U * 4);
  const unsigned short* kp = kc + base + (size_t)(wave * 8 * U + r) * DH + seg * 8;
  const unsigned short* vp = vc + base + (size_t)(wave * 8 * U + r) * DH + seg * 8;
  constexpr size_t BSTR = (size_t)4 * 8 * U * DH;
  float m = -INFINITY, l = 0.f, acc[24];
#pragma unroll
  for (int k = 0; k < 24; ++k) acc[k] = 0.f;
  const float scale = 0.07216878364870322f;
  const float L2E = 1.4426950408889634f;
  u32x4 Kn[U][3], Vn[U][3];
#pragma unroll
  for (int u = 0; u < U; ++u)
#pragma unroll
    for (int i = 0; i < 3; ++i) {
      Kn[u][i] = __builtin_nontemporal_load((const u32x4*)(kp + (size_t)u * 8 * DH + i * 64));
    }
#pragma unroll
  for (int u = 0; u < U; ++u)
#pragma unroll
    for (int i = 0; i < 3; ++i) {
      Vn[u][i] = __builtin_nontemporal_load((const u32x4*)(vp + (size_t)u * 8 * DH + i * 64));
    }
  for (int b = 0; b < nb; ++b) {
    float sc[U];
    const int bn = (b + 1 < nb) ? b + 1 : b;
#pragma unroll
    for (int u = 0; u < U; ++u) {
      float d = 0.f;
#pragma unroll
      for (int i = 0; i < 3; ++i) {
#pragma unroll
        for (int k = 0; k < 4; ++k) {
          d = dot2(Kn[u][i][k], qp[i * 4 + k], d);
          d = dot2(Kn[u][i][k], ql[i * 4 + k], d);
        }
        Kn[u][i] = __builtin_nontemporal_load((const u32x4*)(kp + bn * BSTR + (size_t)u * 8 * DH + i * 64));
      }
      d += dpp<XOR1>(d); d += dpp<XOR2>(d); d += dpp<XOR7>(d);
      sc[u] = rb(rb(d) * scale) * L2E;
    }
    float mn = m;
#pragma unroll
    for (int u = 0; u < U; ++u) mn = fmaxf(mn, sc[u]);
    if (__any(mn > m)) {
      const float al = __builtin_amdgcn_exp2f(m - mn);
      l *= al;
#pragma unroll
      for (int k = 0; k < 24; ++k) acc[k] *= al;
      m = mn;
    }
#pragma unroll
    for (int u = 0; u < U; ++u) {
      const float p = __builtin_amdgcn_exp2f(sc[u] - m);
      l += p;
#pragma unroll
      for (int i = 0; i < 3; ++i) {
#pragma unroll
        for (int k = 0; k < 4; ++k) {
          acc[i * 8 + 2 * k] = fmaf(p, bflo(Vn[u][i][k]), acc[i * 8 + 2 * k]);
          acc[i * 8 + 2 * k + 1] = fmaf(p, bfhi(Vn[u][i][k]), acc[i * 8 + 2 * k + 1]);
        }
        Vn[u][i] = __builtin_nontemporal_load((const u32x4*)(vp + bn * BSTR + (size_t)u * 8 * DH + i * 64));
      }
    }
  }
  float M = m;
  M = fmaxf(M, __shfl_xor(M, 8)); M = fmaxf(M, __shfl_xor(M, 16)); M = fmaxf(M, __shfl_xor(M, 32));
  const float w = __builtin_amdgcn_exp2f(m - M);
  l *= w;
  l += __shfl_xor(l, 8); l += __shfl_xor(l, 16); l += __shfl_xor(l, 32);
#pragma unroll
  for (int k = 0; k < 24; ++k) { float a = acc[k] * w; a += __shfl_xor(a, 8); a += __shfl_xor(a, 16); a += __shfl_xor(a, 32); acc[k] = a; }
  if (r == 0) {
#pragma unroll
    for (int i = 0; i < 3; ++i)
#pragma unroll
      for (int k = 0; k < 8; ++k) macc[wave][i * 64 + seg * 8 + k] = acc[i * 8 + k];
    if (seg == 0) { mrg[wave][0][0] = M; mrg[wave][0][1] = l; }
  }
  __syncthreads();
  if (tid < DH) {
    float MM = -INFINITY;
    for (int w2 = 0; w2 < 4; ++w2) MM = fmaxf(MM, mrg[w2][0][0]);
    float L = 0.f, o = 0.f;
    for (int w2 = 0; w2 < 4; ++w2) { float ww = __builtin_amdgcn_exp2f(mrg[w2][0][0] - MM); L += ww * mrg[w2][0][1]; o += ww * macc[w2][tid]; }
    po[(h * NSPLIT + s) * DH + tid] = o;
    if (tid == 0) { pml[(h * NSPLIT + s) * 2] = MM; pml[(h * NSPLIT + s) * 2 + 1] = L; }
  }
}

__global__ void __launch_bounds__(320) k_wo(const float* pml, const float* po, const unsigned short* wo, float* ypart) {
  __shared__ float o[64];
  __shared__ float part[4][D];
  const int tid = threadIdx.x, r0 = blockIdx.x * 64;
  if (tid < 64) {
    const int row = r0 + tid, h = row / DH, d = row % DH;
    float M = -INFINITY;
    for (int s = 0; s < NSPLIT; ++s) M = fmaxf(M, pml[(h * NSPLIT + s) * 2]);
    float L = 0.f, a = 0.f;
    for (int s = 0; s < NSPLIT; ++s) { float w = __builtin_amdgcn_exp2f(pml[(h * NSPLIT + s) * 2] - M); L += w * pml[(h * NSPLIT + s) * 2 + 1]; a += w * po[(h * NSPLIT + s) * DH + d]; }
    o[tid] = rb(a / L);
  }
  __syncthreads();
  const int cg = tid % 80, rg = tid / 80;
  float acc[8] = {};
#pragma unroll 4
  for (int r = rg; r < 64; r += 4) {
    u32x4 w = *(const u32x4*)(wo + (size_t)(r0 + r) * D + cg * 8);
    float xv = o[r];
    for (int k = 0; k < 4; ++k) { acc[2 * k] += xv * bflo(w[k]); acc[2 * k + 1] += xv * bfhi(w[k]); }
  }
  for (int k = 0; k < 8; ++k) part[rg][cg * 8 + k] = acc[k];
  __syncthreads();
  for (int c = tid; c < D; c += 320) ypart[blockIdx.x * D + c] = part[0][c] + part[1][c] + part[2][c] + part[3][c];
}

__global__ void __launch_bounds__(320) k_mlp(const unsigned short* x, const float* ypart, const unsigned short* wg, const unsigned short* wu,
                                             const unsigned short* wd, float* hout, float* dpart) {
  __shared__ float hs[D], hn[D], red[5], pg[80][32], pu[80][32], act[32], part[4][D];
  const int tid = threadIdx.x, f0 = blockIdx.x * 32;
  for (int c = tid; c < D; c += 320) {
    float s = 0.f;
    for (int j = 0; j < 96; ++j) s += ypart[j * D + c];
    float hv = rb(bf2f(x[c]) + rb(s));
    hs[c] = hv;
    if (blockIdx.x == 0) hout[c] = hv;
  }
  __syncthreads();
  ln640(hs, hn, red, tid, 320);
  {
    const int cg = tid & 3, rg = tid >> 2;
    float ag[8] = {}, au[8] = {};
    for (int r = rg; r < D; r += 80) {
      u32x4 a = *(const u32x4*)(wg + (size_t)r * F + f0 + cg * 8);
      u32x4 b = *(const u32x4*)(wu + (size_t)r * F + f0 + cg * 8);
      float xv = hn[r];
      for (int k = 0; k < 4; ++k) { ag[2 * k] += xv * bflo(a[k]); ag[2 * k + 1] += xv * bfhi(a[k]); au[2 * k] += xv * bflo(b[k]); au[2 * k + 1] += xv * bfhi(b[k]); }
    }
    for (int k = 0; k < 8; ++k) { pg[rg][cg * 8 + k] = ag[k]; pu[rg][cg * 8 + k] = au[k]; }
  }
  __syncthreads();
  if (tid < 32) {
    float g = 0.f, u = 0.f;
    for (int j = 0; j < 80; ++j) { g += pg[j][tid]; u += pu[j][tid]; }
    g = rb(g); u = rb(u);
    float sg = rb(1.f / (1.f + __expf(-g)));
    act[tid] = rb(sg * u);
  }
  __syncthreads();
  const int cg = tid % 80, rg = tid / 80;
  float acc[8] = {};
  for (int r = rg; r < 32; r += 4) {
    u32x4 w = *(const u32x4*)(wd + (size_t)(f0 + r) * D + cg * 8);
    float xv = act[r];
    for (int k = 0; k < 4; ++k) { acc[2 * k] += xv * bflo(w[k]); acc[2 * k + 1] += xv * bfhi(w[k]); }
  }
  for (int k = 0; k < 8; ++k) part[rg][cg * 8 + k] = acc[k];
  __syncthreads();
  for (int c = tid; c < D; c += 320) dpart[blockIdx.x * D + c] = part[0][c] + part[1][c] + part[2][c] + part[3][c];
}

__global__ void __launch_bounds__(64) k_final(const float* hout, const float* dpart, __hip_bfloat16* out) {
  const int c = blockIdx.x * 64 + threadIdx.x;
  float s = 0.f;
  for (int j = 0; j < 48; ++j) s += dpart[j * D + c];
  out[c] = __float2bfloat16(hout[c] + rb(s));
}

extern "C" void kernel_launch(void* const* d_in, const int* in_sizes, int n_in, void* d_out, int out_size, void* d_ws, size_t ws_size,
                              hipStream_t stream) {
  const unsigned short* x = (const unsigned short*)d_in[0];
  const unsigned short* kc = (const unsigned short*)d_in[1];
  const unsigned short* vc = (const unsigned short*)d_in[2];
  const unsigned short* wq = (const unsigned short*)d_in[3];
  const unsigned short* wo = (const unsigned short*)d_in[4];
  const unsigned short* wg = (const unsigned short*)d_in[6];
  const unsigned short* wu = (const unsigned short*)d_in[7];
  const unsigned short* wd = (const unsigned short*)d_in[8];
  float* ws = (float*)d_ws;
  float* q = ws;
  float* pml = ws + 8192;
  float* po = ws + 16384;
  float* ypart = po + H * NSPLIT * DH;
  float* hout = ypart + 96 * D;
  float* dpart = hout + 1024;
  k_qproj<<<96, 256, 0, stream>>>(x, wq, q);
  k_attn<<<dim3(NSPLIT, H), 256, 0, stream>>>(kc, vc, q, pml, po);
  k_wo<<<96, 320, 0, stream>>>(pml, po, wo, ypart);
  k_mlp<<<48, 320, 0, stream>>>(x, ypart, wg, wu, wd, hout, dpart);
  k_final<<<10, 64, 0, stream>>>(hout, dpart, (__hip_bfloat16*)d_out);
}
```

```cpp
#include <hip/hip_runtime.h>
#include <hip/hip_bf16.h>
#include <stdint.h>

#define DIAG 0
#define RING 4
#define WQ_AUX 0
#define W_AUX 2
#define LOOP_BAR 1

namespace {

constexpr int D = 640, H = 32, SK = 32768, DH = 192, F = 1536, HD = H * DH;
constexpr int NWG = 256, NW = 8, NTH = NW * 64, NSPLIT = 8;
constexpr int U = RING;
constexpr int TPW = SK / 8 / (NSPLIT * NW);
constexpr int NCH = TPW / U;
constexpr unsigned TILE_B = 8 * DH * 2;
constexpr unsigned KV_BYTES = (unsigned)H * SK * DH * 2;
constexpr unsigned CH_STRIDE = NSPLIT * NW * U * TILE_B;
constexpr int NMLP = 96, FPW = F / NMLP;
constexpr int NFIN = 40;

constexpr int WS_A = 0;
constexpr int WS_B = WS_A + NWG * 256;
constexpr int WS_D = WS_B + H * D;
constexpr int WS_CLR = WS_D + NMLP * D;
constexpr int WS_DIAG = 1 << 20;
constexpr unsigned EMPTY = 0xFFFFFFFFu;
constexpr unsigned SPIN_CAP = 1u << 20;

constexpr int SC_STR = 68;
constexpr int L_SC = 0;
constexpr int L_RED = L_SC + NW * 8 * SC_STR;
constexpr int L_XN = L_RED + NW * DH;
constexpr int L_Q = L_XN + D;
constexpr int L_ML = L_Q + 96;
constexpr int L_O = L_ML + 16;
constexpr int L_Y = L_O + DH;
constexpr int L_H = L_Y + 3 * D;
constexpr int L_ACT = L_H + D;
constexpr int L_END = L_ACT + 16;
constexpr int LDS_BYTES = 96 * 1024;
static_assert(L_END * 4 <= LDS_BYTES, "lds");

typedef unsigned u32x4 __attribute__((ext_vector_type(4)));
typedef unsigned u32x2 __attribute__((ext_vector_type(2)));
typedef float f32x4 __attribute__((ext_vector_type(4)));
typedef float f32x2 __attribute__((ext_vector_type(2)));
typedef __bf16 bf16x2 __attribute__((ext_vector_type(2)));
#define LAS __attribute__((address_space(3)))
#define GAS __attribute__((address_space(1)))
#define RLX_AGENT __ATOMIC_RELAXED, __HIP_MEMORY_SCOPE_AGENT
#define SB() __builtin_amdgcn_sched_barrier(0)

__device__ __forceinline__ float u2f(unsigned u) { return __builtin_bit_cast(float, u); }
__device__ __forceinline__ unsigned f2u(float f) { return __builtin_bit_cast(unsigned, f); }
__device__ __forceinline__ float bflo(unsigned u) { return u2f(u << 16); }
__device__ __forceinline__ float bfhi(unsigned u) { return u2f(u & 0xffff0000u); }
__device__ __forceinline__ float rb(float x) { return (float)(__bf16)x; }
__device__ __forceinline__ unsigned pack_bf(float lo, float hi) {
  bf16x2 v = {(__bf16)lo, (__bf16)hi};
  return __builtin_bit_cast(unsigned, v);
}
__device__ __forceinline__ float dot2(unsigned k, unsigned q, float acc) {
  return __builtin_amdgcn_fdot2_f32_bf16(__builtin_bit_cast(bf16x2, k), __builtin_bit_cast(bf16x2, q), acc, false);
}
template <int CTRL> __device__ __forceinline__ float dpp(float x) {
  return u2f(__builtin_amdgcn_mov_dpp(f2u(x), CTRL, 0xf, 0xf, true));
}
constexpr int XOR1 = 0xB1, XOR2 = 0x4E, XOR7 = 0x141, ROR8 = 0x128, ROR4 = 0x124;
__device__ __forceinline__ float tr32(float a, float b) {
  auto r = __builtin_amdgcn_permlane32_swap(f2u(a), f2u(b), false, false);
  return u2f(r[0]) + u2f(r[1]);
}
__device__ __forceinline__ float tr16(float a, float b) {
  auto r = __builtin_amdgcn_permlane16_swap(f2u(a), f2u(b), false, false);
  return u2f(r[0]) + u2f(r[1]);
}
__device__ __forceinline__ float wave_sum(float v) {
  v += dpp<XOR1>(v); v += dpp<XOR2>(v); v += dpp<XOR7>(v); v += dpp<ROR8>(v);
  v = tr16(v, v); return tr32(v, v);
}
__device__ __forceinline__ float wave_max(float v) {
  v = fmaxf(v, dpp<XOR1>(v)); v = fmaxf(v, dpp<XOR2>(v)); v = fmaxf(v, dpp<XOR7>(v)); v = fmaxf(v, dpp<ROR8>(v));
  auto r = __builtin_amdgcn_permlane16_swap(f2u(v), f2u(v), false, false);
  v = fmaxf(u2f(r[0]), u2f(r[1]));
  auto t = __builtin_amdgcn_permlane32_swap(f2u(v), f2u(v), false, false);
  return fmaxf(u2f(t[0]), u2f(t[1]));
}
__device__ __forceinline__ void lds_barrier() {
  asm volatile("s_waitcnt lgkmcnt(0)" ::: "memory");
  __builtin_amdgcn_s_barrier();
  asm volatile("" ::: "memory");
}
__device__ __forceinline__ unsigned seal(float v) { const unsigned u = f2u(v); return u < EMPTY ? u : EMPTY - 1; }
__device__ __forceinline__ void pub1(unsigned* p, float v) { __hip_atomic_store((GAS unsigned*)p, seal(v), RLX_AGENT); }
__device__ __forceinline__ unsigned get1(const unsigned* p) { return __hip_atomic_load((GAS unsigned*)p, RLX_AGENT); }
__device__ __forceinline__ bool full4(u32x4 v) { return v[0] != EMPTY && v[1] != EMPTY && v[2] != EMPTY && v[3] != EMPTY; }

__device__ __forceinline__ void axpy8(f32x2* acc, float pv, u32x4 w) {
  const f32x2 pp = {pv, pv};
#pragma unroll
  for (int k = 0; k < 4; ++k) acc[k] = __builtin_elementwise_fma(pp, f32x2{bflo(w[k]), bfhi(w[k])}, acc[k]);
  asm volatile("" : "+v"(acc[0]), "+v"(acc[1]), "+v"(acc[2]), "+v"(acc[3]));
}

__device__ __forceinline__ void reduce24(const f32x2 (&acc)[12], LAS float* red, int lane) {
  float a[12], b[6];
#pragma unroll
  for (int k = 0; k < 12; ++k) a[k] = tr32(acc[k >> 1][k & 1], acc[(k + 12) >> 1][k & 1]);
#pragma unroll
  for (int k = 0; k < 6; ++k) b[k] = tr16(a[k], a[k + 6]);
  const bool up = lane & 8;
  const int k0 = (up ? 3 : 0) + ((lane & 16) ? 6 : 0) + ((lane & 32) ? 12 : 0), seg = lane & 7;
#pragma unroll
  for (int k = 0; k < 3; ++k) {
    const float keep = up ? b[k + 3] : b[k], send = up ? b[k] : b[k + 3];
    const int kk = k0 + k;
    red[(kk >> 3) * 64 + seg * 8 + (kk & 7)] = keep + dpp<ROR8>(send);
  }
}

__device__ __forceinline__ void layernorm10(float (&v)[10]) {
  float s = 0.f;
#pragma unroll
  for (int j = 0; j < 10; ++j) s += v[j];
  const float mean = wave_sum(s) * (1.f / D);
  float q = 0.f;
#pragma unroll
  for (int j = 0; j < 10; ++j) { const float d = v[j] - mean; q = fmaf(d, d, q); }
  const float var = rb(wave_sum(q) * (1.f / D));
  const float m = rb(mean), den = rb(sqrtf(rb(var + 1e-5f)));
#pragma unroll
  for (int j = 0; j < 10; ++j) v[j] = rb(rb(v[j] - m) / den);
}

struct Params {
  const unsigned short *x, *kc, *vc, *wq, *wo, *wg, *wu, *wd;
  unsigned short* out;
  unsigned* ws;
};

#define STAMP(i) do {} while (0)

__global__ void __launch_bounds__(NTH, 2) fused_decode(Params p) {
  extern __shared__ __attribute__((aligned(16))) float smem_[];
  LAS float* sm = (LAS float*)smem_;
  const int tid = threadIdx.x, lane = tid & 63, wave = __builtin_amdgcn_readfirstlane(tid >> 6);
  const int b = blockIdx.x, h = b & 31, s = b >> 5;
  const int r = lane >> 3, seg = lane & 7;
  STAMP(0);

  const auto rk = __builtin_amdgcn_make_buffer_rsrc((void*)p.kc, 0, (int)KV_BYTES, 0x00020000);
  const auto rv = __builtin_amdgcn_make_buffer_rsrc((void*)p.vc, 0, (int)KV_BYTES, 0x00020000);
  const auto rq = __builtin_amdgcn_make_buffer_rsrc((void*)p.wq, 0, D * HD * 2, 0x00020000);
  const unsigned voff = r * (DH * 2) + seg * 16;
  const unsigned so0 = (unsigned)h * (SK * DH * 2) + (s * NW + wave) * (U * TILE_B);

  unsigned xr[5];
#pragma unroll
  for (int j = 0; j < 5; ++j) xr[j] = ((const unsigned*)p.x)[j * 64 + lane];
  SB();
  u32x4 wqr[10][3];
#pragma unroll
  for (int t = 0; t < 10; ++t)
#pragma unroll
    for (int i = 0; i < 3; ++i) {
      wqr[t][i] = __builtin_amdgcn_raw_buffer_load_b128(rq, r * (HD * 2) + seg * 16 + i * 128, (wave * 80 + t * 8) * (HD * 2) + h * (DH * 2), WQ_AUX);
      SB();
    }
  u32x4 ring[U][3];
#pragma unroll
  for (int u = 0; u < U; ++u)
#pragma unroll
    for (int i = 0; i < 3; ++i) {
      ring[u][i] = __builtin_amdgcn_raw_buffer_load_b128(rk, voff + i * 128, so0 + u * TILE_B, 2);
      SB();
    }
  {
    float xv[10];
#pragma unroll
    for (int j = 0; j < 5; ++j) { xv[2 * j] = bflo(xr[j]); xv[2 * j + 1] = bfhi(xr[j]); }
    layernorm10(xv);
#pragma unroll
    for (int j = 0; j < 5; ++j) *(LAS f32x2*)(sm + L_XN + 2 * (j * 64 + lane)) = f32x2{xv[2 * j], xv[2 * j + 1]};
  }
  f32x2 acc[12];
#pragma unroll
  for (int k = 0; k < 12; ++k) acc[k] = f32x2{0.f, 0.f};
  {
    float xn[10];
#pragma unroll
    for (int t = 0; t < 10; ++t) xn[t] = sm[L_XN + wave * 80 + t * 8 + r];
#pragma unroll
    for (int t = 0; t < 10; ++t)
#pragma unroll
      for (int i = 0; i < 3; ++i) axpy8(acc + i * 4, xn[t], wqr[t][i]);
  }
  reduce24(acc, sm + L_RED + wave * DH, lane);
  lds_barrier();
  if (tid < 96) {
    f32x2 t = {0.f, 0.f};
#pragma unroll
    for (int w = 0; w < NW; ++w) t += *(LAS f32x2*)(sm + L_RED + w * DH + 2 * tid);
    ((LAS unsigned*)sm)[L_Q + tid] = pack_bf(t[0], t[1]);
  }
  lds_barrier();
  u32x4 qp[3];
#pragma unroll
  for (int i = 0; i < 3; ++i) qp[i] = *(LAS u32x4*)(sm + L_Q + i * 32 + seg * 4);
  STAMP(1);

  LAS float* sc = sm + L_SC + (wave * 8 + r) * SC_STR;
  const float scale = 0.07216878364870322f;
#define K_CHUNK(c_, RS, so_)                                                                       \
  do {                                                                                             \
    float mine = 0.f;                                                                              \
    _Pragma("unroll") for (int u = 0; u < U; ++u) {                                                \
      float d0 = 0.f, d1 = 0.f;                                                                    \
      _Pragma("unroll") for (int i = 0; i < 3; ++i) {                                              \
        d0 = dot2(ring[u][i][0], qp[i][0], d0); d1 = dot2(ring[u][i][1], qp[i][1], d1);            \
        d0 = dot2(ring[u][i][2], qp[i][2], d0); d1 = dot2(ring[u][i][3], qp[i][3], d1);            \
        ring[u][i] = __builtin_amdgcn_raw_buffer_load_b128(RS, voff + i * 128, (so_) + u * TILE_B, 2); \
        SB();                                                                                      \
      }                                                                                            \
      float d = d0 + d1;                                                                           \
      d += dpp<XOR1>(d); d += dpp<XOR2>(d); d += dpp<XOR7>(d);                                     \
      mine = ((seg & (U - 1)) == u) ? d : mine;                                                    \
    }                                                                                              \
    sc[(c_) * U + (seg & (U - 1))] = rb(rb(mine) * scale);                                         \
  } while (0)
  {
    unsigned so = so0;
    for (int c = 0; c < NCH - 1; ++c) {
      so += CH_STRIDE;
      K_CHUNK(c, rk, so);
      if (LOOP_BAR) __builtin_amdgcn_s_barrier();
    }
    K_CHUNK(NCH - 1, rv, so0);
  }
  STAMP(2);
  float Mwg;
  {
    f32x4 s0 = *(LAS f32x4*)(sc + seg * 8), s1 = *(LAS f32x4*)(sc + seg * 8 + 4);
    float m = fmaxf(fmaxf(fmaxf(s0[0], s0[1]), fmaxf(s0[2], s0[3])), fmaxf(fmaxf(s1[0], s1[1]), fmaxf(s1[2], s1[3])));
    m = wave_max(m);
    if (lane == 0) sm[L_ML + wave] = m;
    lds_barrier();
    const f32x4 m0 = *(LAS f32x4*)(sm + L_ML), m1 = *(LAS f32x4*)(sm + L_ML + 4);
    Mwg = fmaxf(fmaxf(fmaxf(m0[0], m0[1]), fmaxf(m0[2], m0[3])), fmaxf(fmaxf(m1[0], m1[1]), fmaxf(m1[2], m1[3])));
    const float L2E = 1.4426950408889634f;
    float l = 0.f;
#pragma unroll
    for (int k = 0; k < 4; ++k) {
      s0[k] = __builtin_amdgcn_exp2f((s0[k] - Mwg) * L2E); s1[k] = __builtin_amdgcn_exp2f((s1[k] - Mwg) * L2E);
      l += s0[k] + s1[k];
    }
    *(LAS f32x4*)(sc + seg * 8) = s0; *(LAS f32x4*)(sc + seg * 8 + 4) = s1;
    l = wave_sum(l);
    if (lane == 0) sm[L_ML + 8 + wave] = l;
  }
#pragma unroll
  for (int k = 0; k < 12; ++k) acc[k] = f32x2{0.f, 0.f};
#define V_TILE(u, pv) axpy8(acc + i * 4, pv, ring[u][i])
  {
    unsigned so = so0;
    for (int c = 0; c < NCH - 1; ++c) {
      so += CH_STRIDE;
      float pv[U];
#pragma unroll
      for (int u = 0; u < U; u += 4) { const f32x4 t = *(LAS f32x4*)(sc + c * U + u); pv[u] = t[0]; pv[u + 1] = t[1]; pv[u + 2] = t[2]; pv[u + 3] = t[3]; }
#pragma unroll
      for (int u = 0; u < U; ++u)
#pragma unroll
        for (int i = 0; i < 3; ++i) {
          V_TILE(u, pv[u]);
          ring[u][i] = __builtin_amdgcn_raw_buffer_load_b128(rv, voff + i * 128, so + u * TILE_B, 2);
          SB();
        }
      if (LOOP_BAR) lds_barrier();
    }
  }
  const int rg4 = lane >> 4, cg10 = lane & 15;
  u32x4 wor[6];
  {
    const auto ro = __builtin_amdgcn_make_buffer_rsrc((void*)p.wo, 0, HD * D * 2, 0x00020000);
    const unsigned vo = cg10 < 10 ? (unsigned)(rg4 * 6 * (D * 2) + cg10 * 16) : 0x80000000u;
#pragma unroll
    for (int j = 0; j < 6; ++j) {
      wor[j] = __builtin_amdgcn_raw_buffer_load_b128(ro, vo, (h * DH + wave * 24 + j) * (D * 2) + s * 160, W_AUX);
      SB();
    }
  }
  const int mj = (b & 7) * 12 + (b >> 3);
  const bool is_mlp = (b >> 3) < 12;
  u32x4 wgu[5];
  unsigned wdr[16];
  const int gmat = wave & 1, gcg = lane & 1, grg = (lane >> 1) + 32 * (wave >> 1);
  {
    const auto rg_ = __builtin_amdgcn_make_buffer_rsrc((void*)p.wg, 0, D * F * 2, 0x00020000);
    const auto ru_ = __builtin_amdgcn_make_buffer_rsrc((void*)p.wu, 0, D * F * 2, 0x00020000);
    const auto rd_ = __builtin_amdgcn_make_buffer_rsrc((void*)p.wd, 0, D * F * 2, 0x00020000);
    const unsigned vo = is_mlp ? (unsigned)(grg * (F * 2) + mj * (FPW * 2) + gcg * 16) : 0x80000000u;
#pragma unroll
    for (int j = 0; j < 5; ++j) {
      wgu[j] = __builtin_amdgcn_raw_buffer_load_b128(gmat ? ru_ : rg_, vo, j * 128 * (F * 2), W_AUX);
      SB();
    }
    const unsigned vd = (is_mlp && wave < 5) ? (unsigned)(mj * FPW * (D * 2) + tid * 4) : 0x80000000u;
#pragma unroll
    for (int j = 0; j < 16; ++j) { wdr[j] = __builtin_amdgcn_raw_buffer_load_b32(rd_, vd, j * (D * 2), W_AUX); SB(); }
  }
  {
    float pv[U];
#pragma unroll
    for (int u = 0; u < U; u += 4) { const f32x4 t = *(LAS f32x4*)(sc + (NCH - 1) * U + u); pv[u] = t[0]; pv[u + 1] = t[1]; pv[u + 2] = t[2]; pv[u + 3] = t[3]; }
#pragma unroll
    for (int u = 0; u < U; ++u)
#pragma unroll
      for (int i = 0; i < 3; ++i) { V_TILE(u, pv[u]); SB(); }
  }
  STAMP(3);
  reduce24(acc, sm + L_RED + wave * DH, lane);
  lds_barrier();
  unsigned* wsA = p.ws + WS_A;
  if (wave < 3) {
    float t = 0.f;
#pragma unroll
    for (int w = 0; w < NW; ++w) t += sm[L_RED + w * DH + tid];
    pub1(wsA + b * 256 + tid, t);
    if (tid == 0) {
      float l = 0.f;
#pragma unroll
      for (int w = 0; w < NW; ++w) l += sm[L_ML + 8 + w];
      pub1(wsA + b * 256 + 192, Mwg);
      pub1(wsA + b * 256 + 193, l);
    }
    STAMP(4);
    unsigned v[NSPLIT], mlv;
    for (unsigned spins = 0; spins < SPIN_CAP; ++spins) {
      mlv = get1(wsA + (h + 32 * (lane & 7)) * 256 + 192 + ((lane >> 3) & 1));
      bool ok = mlv != EMPTY;
#pragma unroll
      for (int j = 0; j < NSPLIT; ++j) { v[j] = get1(wsA + (h + 32 * j) * 256 + tid); ok &= v[j] != EMPTY; }
      if (__all(ok)) break;
      __builtin_amdgcn_s_sleep(1);
    }
    const float ml = u2f(mlv);
    float M = ml;
    M = fmaxf(M, dpp<XOR1>(M)); M = fmaxf(M, dpp<XOR2>(M)); M = fmaxf(M, dpp<XOR7>(M));
    const float wgt = __builtin_amdgcn_exp2f((ml - M) * 1.4426950408889634f);
    float L = wgt * dpp<ROR8>(ml);
    L += dpp<XOR1>(L); L += dpp<XOR2>(L); L += dpp<XOR7>(L);
    float o = 0.f;
#pragma unroll
    for (int j = 0; j < NSPLIT; ++j) o = fmaf(u2f(__builtin_amdgcn_readlane(f2u(wgt), j)), u2f(v[j]), o);
    sm[L_O + tid] = rb(o / u2f(__builtin_amdgcn_readlane(f2u(L), 0)));
  }
  lds_barrier();
  STAMP(5);
  {
    float y[8];
#pragma unroll
    for (int k = 0; k < 8; ++k) y[k] = 0.f;
#pragma unroll
    for (int j = 0; j < 6; ++j) {
      const float ov = sm[L_O + wave * 24 + rg4 * 6 + j];
#pragma unroll
      for (int k = 0; k < 4; ++k) { y[2 * k] = fmaf(ov, bflo(wor[j][k]), y[2 * k]); y[2 * k + 1] = fmaf(ov, bfhi(wor[j][k]), y[2 * k + 1]); }
    }
    float a[4];
#pragma unroll
    for (int k = 0; k < 4; ++k) a[k] = tr32(y[k], y[k + 4]);
    const f32x2 e = {tr16(a[0], a[2]), tr16(a[1], a[3])};
    if (cg10 < 10) *(LAS f32x2*)(sm + L_RED + wave * 80 + cg10 * 8 + ((lane & 16) ? 2 : 0) + ((lane & 32) ? 4 : 0)) = e;
  }
  lds_barrier();
  if (tid < 80) {
    float t = 0.f;
#pragma unroll
    for (int w = 0; w < NW; ++w) t += sm[L_RED + w * 80 + tid];
    pub1(p.ws + WS_B + h * D + s * 80 + tid, t);
  }
  STAMP(6);
  if (!is_mlp) return;
  {
    const auto rb_ = __builtin_amdgcn_make_buffer_rsrc((void*)(p.ws + WS_B), 0, H * D * 4, 0x00020000);
    const int part = tid / 160, cq = tid - part * 160;
    u32x4 v[11];
    bool need[11];
#pragma unroll
    for (int j = 0; j < 11; ++j) { need[j] = true; v[j] = u32x4{0u, 0u, 0u, 0u}; }
    for (unsigned spins = 0; spins < SPIN_CAP; ++spins) {
#pragma unroll
      for (int j = 0; j < 11; ++j) {
        const int hh = part + 3 * j;
        const unsigned vo = (part < 3 && hh < H) ? (unsigned)(hh * D * 4 + cq * 16) : 0x80000000u;
        if (need[j]) v[j] = __builtin_amdgcn_raw_buffer_load_b128(rb_, vo, 0, 16);
      }
      bool any = false;
#pragma unroll
      for (int j = 0; j < 11; ++j) { need[j] = __any(!full4(v[j])); any |= need[j]; }
      if (!any) break;
      __builtin_amdgcn_s_sleep(1);
    }
    f32x4 t = {0.f, 0.f, 0.f, 0.f};
#pragma unroll
    for (int j = 0; j < 11; ++j) t += __builtin_bit_cast(f32x4, v[j]);
    if (part < 3) *(LAS f32x4*)(sm + L_Y + part * D + cq * 4) = t;
  }
  lds_barrier();
  STAMP(7);
  {
    float hv[10];
#pragma unroll
    for (int j = 0; j < 5; ++j) {
      const int c = 2 * (j * 64 + lane);
      const f32x2 y = *(LAS f32x2*)(sm + L_Y + c) + *(LAS f32x2*)(sm + L_Y + D + c) + *(LAS f32x2*)(sm + L_Y + 2 * D + c);
      hv[2 * j] = rb(bflo(xr[j]) + rb(y[0])); hv[2 * j + 1] = rb(bfhi(xr[j]) + rb(y[1]));
      *(LAS f32x2*)(sm + L_H + c) = f32x2{hv[2 * j], hv[2 * j + 1]};
    }
    layernorm10(hv);
#pragma unroll
    for (int j = 0; j < 5; ++j) *(LAS f32x2*)(sm + L_XN + 2 * (j * 64 + lane)) = f32x2{hv[2 * j], hv[2 * j + 1]};
    float g[8];
#pragma unroll
    for (int k = 0; k < 8; ++k) g[k] = 0.f;
#pragma unroll
    for (int j = 0; j < 5; ++j) {
      const float xn = sm[L_XN + j * 128 + grg];
#pragma unroll
      for (int k = 0; k < 4; ++k) { g[2 * k] = fmaf(xn, bflo(wgu[j][k]), g[2 * k]); g[2 * k + 1] = fmaf(xn, bfhi(wgu[j][k]), g[2 * k + 1]); }
    }
    float a[4];
#pragma unroll
    for (int k = 0; k < 4; ++k) a[k] = tr32(g[k], g[k + 4]);
    f32x2 e = {tr16(a[0], a[2]), tr16(a[1], a[3])};
    e[0] += dpp<ROR8>(e[0]); e[1] += dpp<ROR8>(e[1]);
    e[0] += dpp<ROR4>(e[0]); e[1] += dpp<ROR4>(e[1]);
    e[0] += dpp<XOR2>(e[0]); e[1] += dpp<XOR2>(e[1]);
    if ((lane & 14) == 0) *(LAS f32x2*)(sm + L_RED + (wave >> 1) * 32 + gmat * 16 + gcg * 8 + ((lane & 16) ? 2 : 0) + ((lane & 32) ? 4 : 0)) = e;
  }
  lds_barrier();
  if (wave < 5) {
    float t = 0.f;
#pragma unroll
    for (int w = 0; w < 4; ++w) t += sm[L_RED + w * 32 + (lane & 31)];
    t = rb(t);
    auto sw = __builtin_amdgcn_permlane16_swap(f2u(t), f2u(t), false, false);
    const float gt = u2f(sw[0]), up = u2f(sw[1]);
    const float act = rb(rb(1.f / (1.f + __expf(-gt))) * up);
    float d0 = 0.f, d1 = 0.f;
#pragma unroll
    for (int j = 0; j < 16; ++j) {
      const float av = u2f(__builtin_amdgcn_readlane(f2u(act), j));
      d0 = fmaf(av, bflo(wdr[j]), d0); d1 = fmaf(av, bfhi(wdr[j]), d1);
    }
    __hip_atomic_store((GAS unsigned long long*)(p.ws + WS_D + mj * D + 2 * tid), ((unsigned long long)seal(d1) << 32) | seal(d0), RLX_AGENT);
  }
  STAMP(8);
  if (mj >= NFIN) return;
  if (wave < 6) {
    const auto rd = __builtin_amdgcn_make_buffer_rsrc((void*)(p.ws + WS_D), 0, NMLP * D * 4, 0x00020000);
    u32x4 v;
    for (unsigned spins = 0; spins < SPIN_CAP; ++spins) {
      v = __builtin_amdgcn_raw_buffer_load_b128(rd, (tid >> 2) * (D * 4) + mj * 64 + (tid & 3) * 16, 0, 16);
      if (__all(full4(v))) break;
      __builtin_amdgcn_s_sleep(1);
    }
    float e = tr16(tr32(u2f(v[0]), u2f(v[2])), tr32(u2f(v[1]), u2f(v[3])));
    e += dpp<ROR8>(e); e += dpp<ROR4>(e);
    if ((lane & 12) == 0) sm[L_Y + wave * 16 + (lane & 3) * 4 + ((lane & 16) ? 1 : 0) + ((lane & 32) ? 2 : 0)] = e;
  }
  lds_barrier();
  if (tid < 16) {
    float t = 0.f;
#pragma unroll
    for (int w = 0; w < 6; ++w) t += sm[L_Y + w * 16 + tid];
    const __bf16 o = (__bf16)(sm[L_H + mj * 16 + tid] + rb(t));
    p.out[mj * 16 + tid] = __builtin_bit_cast(unsigned short, o);
  }
  STAMP(9);
}

}

extern "C" void kernel_launch(void* const* d_in, const int* in_sizes, int n_in, void* d_out, int out_size, void* d_ws, size_t ws_size,
                              hipStream_t stream) {
  Params p;
  p.x = (const unsigned short*)d_in[0];
  p.kc = (const unsigned short*)d_in[1];
  p.vc = (const unsigned short*)d_in[2];
  p.wq = (const unsigned short*)d_in[3];
  p.wo = (const unsigned short*)d_in[4];
  p.wg = (const unsigned short*)d_in[6];
  p.wu = (const unsigned short*)d_in[7];
  p.wd = (const unsigned short*)d_in[8];
  p.out = (unsigned short*)d_out;
  p.ws = (unsigned*)d_ws;
  (void)hipFuncSetAttribute((const void*)fused_decode, hipFuncAttributeMaxDynamicSharedMemorySize, LDS_BYTES);
  (void)hipMemsetAsync(d_ws, 0xFF, (size_t)WS_CLR * 4, stream);
  fused_decode<<<NWG, NTH, LDS_BYTES, stream>>>(p);
}
```

```cpp
#include <hip/hip_runtime.h>
#include <hip/hip_bf16.h>
#include <stdint.h>

#define DIAG 0
#define UT 4
#define RC 1
#define B1 2
#define QSLEEP 16
#define DYN_ROLE 1
#define WQ_AUX 0
#define W_AUX 0
#define WO_FIRST 1
#define WIDE_D 1
#define LOOP_BAR 1

namespace {

constexpr int D = 640, H = 32, SK = 32768, DH = 192, F = 1536, HD = H * DH;
constexpr int NWG = 256, NW = 8, NTH = NW * 64, NSPLIT = 8;
constexpr int U = UT;
constexpr int RD = UT * RC;
constexpr int TPW = SK / 8 / (NSPLIT * NW);
constexpr int NIT = TPW / RD;
constexpr unsigned TILE_B = 8 * DH * 2;
constexpr unsigned KV_BYTES = (unsigned)H * SK * DH * 2;
constexpr unsigned CH_STRIDE = NSPLIT * NW * U * TILE_B;
constexpr int NMLP = 96, FPW = F / NMLP;
constexpr int NFIN = 40;

constexpr int WS_E = 0;
constexpr int WS_T = 64;
constexpr int WS_C = 128;
constexpr int WS_Q = 1024;
constexpr int WS_A = WS_Q + NWG * 2 * DH;
constexpr int WS_B = WS_A + NWG * 256;
constexpr int WS_D = WS_B + H * D;
constexpr int WS_END = WS_D + NMLP * D;
constexpr int WS_DIAG = 1 << 20;
constexpr unsigned OOB = 0x80000000u;
constexpr unsigned EMPTY = 0xFFFFFFFFu;
constexpr unsigned SPIN_CAP = 1u << 20;

constexpr int SC_STR = 68;
constexpr int L_SC = 0;
constexpr int L_RED = L_SC + NW * 8 * SC_STR;
constexpr int L_Q = L_RED + 16 * DH;
constexpr int L_ML = L_Q + 96;
constexpr int L_O = L_ML + 20;
constexpr int L_H = L_O + 96;
constexpr int L_XN = L_H + 320;
constexpr int L_GU = L_XN + 320;
constexpr int L_W = L_GU + 160;
constexpr int L_END = L_W + 5 * 2048;
constexpr int LDS_BYTES = 96 * 1024;
static_assert(L_END * 4 <= LDS_BYTES && L_W % 4 == 0 && L_O % 4 == 0 && L_XN % 4 == 0 && L_Q % 4 == 0, "lds");

typedef unsigned u32x4 __attribute__((ext_vector_type(4)));
typedef unsigned u32x2 __attribute__((ext_vector_type(2)));
typedef float f32x4 __attribute__((ext_vector_type(4)));
typedef float f32x2 __attribute__((ext_vector_type(2)));
typedef __bf16 bf16x2 __attribute__((ext_vector_type(2)));
typedef __bf16 bf16x8 __attribute__((ext_vector_type(8)));
typedef short s16x4 __attribute__((ext_vector_type(4)));
typedef short s16x8 __attribute__((ext_vector_type(8)));
#define LAS __attribute__((address_space(3)))
#define GAS __attribute__((address_space(1)))
#define CAS __attribute__((address_space(4)))
#define RLX_AGENT __ATOMIC_RELAXED, __HIP_MEMORY_SCOPE_AGENT
#define SB() __builtin_amdgcn_sched_barrier(0)
#define PIN(x) asm volatile("" : "+v"(x))
#define REPOLL() asm volatile("s_sleep 1" ::: "memory")
#define SPIN _Pragma("nounroll") for (unsigned spins = 0; spins < SPIN_CAP; ++spins)
constexpr int SC1 = 16, NT = 2;
constexpr float L2E = 1.4426950408889634f;

__device__ __forceinline__ float u2f(unsigned u) { return __builtin_bit_cast(float, u); }
__device__ __forceinline__ unsigned f2u(float f) { return __builtin_bit_cast(unsigned, f); }
__device__ __forceinline__ float bflo(unsigned u) { return u2f(u << 16); }
__device__ __forceinline__ float bfhi(unsigned u) { return u2f(u & 0xffff0000u); }
__device__ __forceinline__ float rb(float x) { return (float)(__bf16)x; }
__device__ __forceinline__ unsigned pack_bf(float lo, float hi) {
  bf16x2 v = {(__bf16)lo, (__bf16)hi};
  return __builtin_bit_cast(unsigned, v);
}
__device__ __forceinline__ float dot2(unsigned k, unsigned q, float acc) {
  return __builtin_amdgcn_fdot2_f32_bf16(__builtin_bit_cast(bf16x2, k), __builtin_bit_cast(bf16x2, q), acc, false);
}
template <int CTRL> __device__ __forceinline__ float dpp(float x) {
  return u2f(__builtin_amdgcn_mov_dpp(f2u(x), CTRL, 0xf, 0xf, true));
}
constexpr int XOR1 = 0xB1, XOR2 = 0x4E, XOR7 = 0x141, ROR8 = 0x128, ROR4 = 0x124;
__device__ __forceinline__ float lane_val(float v, int l) { return u2f(__builtin_amdgcn_readlane(f2u(v), l)); }
__device__ __forceinline__ float tr32(float a, float b) {
  auto r = __builtin_amdgcn_permlane32_swap(f2u(a), f2u(b), false, false);
  return u2f(r[0]) + u2f(r[1]);
}
__device__ __forceinline__ float tr16(float a, float b) {
  auto r = __builtin_amdgcn_permlane16_swap(f2u(a), f2u(b), false, false);
  return u2f(r[0]) + u2f(r[1]);
}
__device__ __forceinline__ float wave_sum(float v) {
  v += dpp<XOR1>(v); v += dpp<XOR2>(v); v += dpp<XOR7>(v); v += dpp<ROR8>(v);
  v += u2f(__builtin_amdgcn_update_dpp(0, f2u(v), 0x142, 0xa, 0xf, false));
  v += u2f(__builtin_amdgcn_update_dpp(0, f2u(v), 0x143, 0xc, 0xf, false));
  return lane_val(v, 63);
}
__device__ __forceinline__ float wave_max(float v) {
  v = fmaxf(v, dpp<XOR1>(v)); v = fmaxf(v, dpp<XOR2>(v)); v = fmaxf(v, dpp<XOR7>(v)); v = fmaxf(v, dpp<ROR8>(v));
  auto r = __builtin_amdgcn_permlane16_swap(f2u(v), f2u(v), false, false);
  v = fmaxf(u2f(r[0]), u2f(r[1]));
  auto t = __builtin_amdgcn_permlane32_swap(f2u(v), f2u(v), false, false);
  return fmaxf(u2f(t[0]), u2f(t[1]));
}
__device__ __forceinline__ void lds_barrier() {
  asm volatile("s_waitcnt lgkmcnt(0)" ::: "memory");
  __builtin_amdgcn_s_barrier();
  asm volatile("" ::: "memory");
}
__device__ __forceinline__ unsigned umax(unsigned a, unsigned b) { return a > b ? a : b; }
__device__ __forceinline__ unsigned umax4(u32x4 v) { return umax(umax(v[0], v[1]), umax(v[2], v[3])); }
__device__ __forceinline__ unsigned seal(float v) { const unsigned u = f2u(v); return u < EMPTY - 1 ? u : EMPTY - 1; }
__device__ __forceinline__ bool none_empty(unsigned mx) { return __builtin_amdgcn_ballot_w64(mx == EMPTY) == 0; }

template <int SITE> __device__ __forceinline__ void axpy8(f32x2* acc, float pv, u32x4 w) {
  asm volatile("; site %1" : "+v"(w) : "n"(SITE));
  const f32x2 pp = {pv, pv};
#pragma unroll
  for (int k = 0; k < 4; ++k) acc[k] = __builtin_elementwise_fma(pp, f32x2{bflo(w[k]), bfhi(w[k])}, acc[k]);
  asm volatile("" : "+v"(acc[0]), "+v"(acc[1]), "+v"(acc[2]), "+v"(acc[3]));
}

__device__ __forceinline__ void ln_stats(const unsigned (&pr)[5], float& m, float& rinv) {
  float v[10], s = 0.f;
#pragma unroll
  for (int j = 0; j < 5; ++j) { v[2 * j] = bflo(pr[j]); v[2 * j + 1] = bfhi(pr[j]); }
  s = ((v[0] + v[1]) + (v[2] + v[3])) + ((v[4] + v[5]) + (v[6] + v[7])) + (v[8] + v[9]);
  const float mean = wave_sum(s) * (1.f / D);
  float q0 = 0.f, q1 = 0.f;
#pragma unroll
  for (int j = 0; j < 5; ++j) { const float d0 = v[2 * j] - mean, d1 = v[2 * j + 1] - mean; q0 = fmaf(d0, d0, q0); q1 = fmaf(d1, d1, q1); }
  const float var = rb(wave_sum(q0 + q1) * (1.f / D));
  m = rb(mean);
  rinv = __builtin_amdgcn_rcpf(rb(__builtin_amdgcn_sqrtf(rb(var + 1e-5f))));
}

__device__ __forceinline__ bf16x8 frag_tr(LAS unsigned* img, int lane, int t) {
  LAS unsigned char* a = (LAS unsigned char*)img + (32 * t + 8 * (lane >> 4) + ((lane >> 2) & 3)) * 32 + (lane & 3) * 8;
  const s16x4 lo = __builtin_amdgcn_ds_read_tr16_b64_v4i16((LAS s16x4*)a);
  const s16x4 hi = __builtin_amdgcn_ds_read_tr16_b64_v4i16((LAS s16x4*)(a + 128));
  return __builtin_bit_cast(bf16x8, __builtin_shufflevector(lo, hi, 0, 1, 2, 3, 4, 5, 6, 7));
}
__device__ __forceinline__ bf16x8 frag_vec(LAS unsigned* vec, int lane, int t) {
  return __builtin_bit_cast(bf16x8, *(LAS u32x4*)(vec + 16 * t + 4 * (lane >> 4)));
}

struct Params {
  const unsigned short *x, *kc, *vc, *wq, *wo, *wg, *wu, *wd;
  unsigned short* out;
  unsigned* ws;
};

#define STAMP(i) asm volatile("; MARK " #i)
#define STAMP0(i) do { if (wave == 0) STAMP(i); } while (0)
#define STAMP3(i) do { if (wave == 3) STAMP(i); } while (0)

__global__ void __launch_bounds__(NTH, 2) fused_decode(Params p) {
  extern __shared__ __attribute__((aligned(16))) float smem_[];
  LAS float* sm = (LAS float*)smem_;
  LAS unsigned* su = (LAS unsigned*)smem_;
  const int tid = threadIdx.x, lane = tid & 63, wave = __builtin_amdgcn_readfirstlane(tid >> 6);
  const int b = blockIdx.x, h = b & 31, s = b >> 5;
  const int r = lane >> 3, seg = lane & 7;
  STAMP0(0);

  const auto rk = __builtin_amdgcn_make_buffer_rsrc((void*)p.kc, 0, (int)KV_BYTES, 0x00020000);
  const auto rv = __builtin_amdgcn_make_buffer_rsrc((void*)p.vc, 0, (int)KV_BYTES, 0x00020000);
  const auto rq = __builtin_amdgcn_make_buffer_rsrc((void*)p.wq, 0, D * HD * 2, 0x00020000);
  const auto rw = __builtin_amdgcn_make_buffer_rsrc((void*)p.ws, 0, WS_END * 4, 0x00020000);
  const unsigned voff = r * (DH * 2) + seg * 16;
  const unsigned so0 = (unsigned)h * (SK * DH * 2) + (s * NW + wave) * (U * TILE_B);
#define SLOT_OFF(it_, j_) (so0 + ((it_) * RC + (j_) / U) * CH_STRIDE + ((j_) % U) * TILE_B)

  unsigned xr[5];
#pragma unroll
  for (int j = 0; j < 5; ++j) xr[j] = ((const unsigned*)p.x)[j * 64 + lane];
  SB();
  const int qrr = lane >= 24, qsg = lane - 24 * qrr;
  u32x4 wqr[5];
#pragma unroll
  for (int j = 0; j < 5; ++j) {
    wqr[j] = __builtin_amdgcn_raw_buffer_load_b128(rq, lane < 48 ? qrr * (HD * 2) + qsg * 16 : OOB, (s * 80 + wave * 10 + 2 * j) * (HD * 2) + h * (DH * 2), WQ_AUX);
    SB();
  }
  const unsigned long long ep = __hip_atomic_load((GAS unsigned long long*)(p.ws + WS_E), RLX_AGENT);
  SB();
  if (wave == NW - 1) {
    const u32x4 e4 = {EMPTY, EMPTY, EMPTY, EMPTY};
    __builtin_amdgcn_raw_buffer_store_b128(e4, rw, lane < 49 ? lane * 16 : OOB, (WS_A + b * 256) * 4, SC1);
    __builtin_amdgcn_raw_buffer_store_b128(e4, rw, lane < 20 ? lane * 16 : OOB, (WS_B + h * D + s * 80) * 4, SC1);
    if (b < NMLP) {
#pragma unroll
      for (int j = 0; j < 3; ++j) __builtin_amdgcn_raw_buffer_store_b128(e4, rw, j * 64 + lane < 160 ? (j * 64 + lane) * 16 : OOB, (WS_D + b * D) * 4, SC1);
    }
    if (b == 0) __builtin_amdgcn_raw_buffer_store_b32(0u, rw, lane == 0 ? 0 : OOB, WS_T * 4, SC1);
  }
  SB();
  __builtin_amdgcn_s_barrier();
  u32x4 ring[RD][3];
#pragma unroll
  for (int j = 0; j < B1; ++j)
#pragma unroll
    for (int i = 0; i < 3; ++i) {
      ring[j][i] = __builtin_amdgcn_raw_buffer_load_b128(rk, voff + i * 128, SLOT_OFF(0, j), NT);
      SB();
    }
  {
    const CAS unsigned* xs = (const CAS unsigned*)p.x + s * 40 + wave * 5;
    float m, rinv;
    ln_stats(xr, m, rinv);
    f32x2 a4[4];
#pragma unroll
    for (int k = 0; k < 4; ++k) a4[k] = f32x2{0.f, 0.f};
#pragma unroll
    for (int j = 0; j < 5; ++j) {
      const unsigned xp = xs[j];
      const float xv = qrr ? bfhi(xp) : bflo(xp);
      axpy8<0>(a4, rb(rb(xv - m) * rinv), wqr[j]);
    }
    if (lane < 48) {
      LAS float* dst = sm + L_RED + (wave * 2 + qrr) * DH + qsg * 8;
      *(LAS f32x4*)dst = f32x4{a4[0][0], a4[0][1], a4[1][0], a4[1][1]};
      *(LAS f32x4*)(dst + 4) = f32x4{a4[2][0], a4[2][1], a4[3][0], a4[3][1]};
    }
  }
  const unsigned tag_lo = __builtin_amdgcn_readfirstlane((unsigned)(ep + 1)), tag_hi = __builtin_amdgcn_readfirstlane((unsigned)((ep + 1) >> 32));
  lds_barrier();
  STAMP0(10);
  if (wave < 3) {
    float t0 = 0.f, t1 = 0.f;
#pragma unroll
    for (int w = 0; w < 16; w += 2) { t0 += sm[L_RED + w * DH + tid]; t1 += sm[L_RED + (w + 1) * DH + tid]; }
    __builtin_amdgcn_raw_buffer_store_b64(u32x2{f2u(t0 + t1), tag_lo}, rw, tid * 8, WS_Q * 4 + b * (DH * 8), SC1);
  }
  if (wave == NW - 1) {
    asm volatile("s_waitcnt vmcnt(%0)" ::"n"(3 * B1) : "memory");
    __builtin_amdgcn_raw_buffer_store_b64(u32x2{tag_lo, tag_hi}, rw, lane == 0 ? 0 : OOB, WS_C * 4 + b * 8, SC1);
  }
  __builtin_amdgcn_s_sleep(QSLEEP);
  {
    u32x2 g[NSPLIT];
    const unsigned vq = wave < 3 ? (unsigned)tid * 8 : OOB;
#pragma unroll
    for (int j = 0; j < NSPLIT; ++j) { g[j] = __builtin_amdgcn_raw_buffer_load_b64(rw, vq, WS_Q * 4 + (h + 32 * j) * (DH * 8), SC1); SB(); }
    __builtin_amdgcn_s_barrier();
#pragma unroll
    for (int j = B1; j < RD; ++j)
#pragma unroll
      for (int i = 0; i < 3; ++i) {
        ring[j][i] = __builtin_amdgcn_raw_buffer_load_b128(rk, voff + i * 128, SLOT_OFF(0, j), NT);
        SB();
      }
    if (wave < 3) {
      bool ok = true;
#pragma unroll
      for (int j = 0; j < NSPLIT; ++j) ok &= g[j][1] == tag_lo;
      if (!__all(ok)) {
        STAMP0(11);
        SPIN {
          REPOLL();
          ok = true;
#pragma unroll
          for (int j = 0; j < NSPLIT; ++j) { g[j] = __builtin_amdgcn_raw_buffer_load_b64(rw, vq, WS_Q * 4 + (h + 32 * j) * (DH * 8), SC1); ok &= g[j][1] == tag_lo; }
          if (__all(ok)) break;
        }
      }
      const float t = ((u2f(g[0][0]) + u2f(g[1][0])) + (u2f(g[2][0]) + u2f(g[3][0]))) + ((u2f(g[4][0]) + u2f(g[5][0])) + (u2f(g[6][0]) + u2f(g[7][0])));
      const float tn = dpp<XOR1>(t);
      if (!(lane & 1)) su[L_Q + (tid >> 1)] = pack_bf(t, tn);
    }
  }
  lds_barrier();
  u32x4 qp[3];
#pragma unroll
  for (int i = 0; i < 3; ++i) qp[i] = *(LAS u32x4*)(su + L_Q + i * 32 + seg * 4);
  STAMP0(1);

  LAS float* sc = sm + L_SC + (wave * 8 + r) * SC_STR;
  const float scale = 0.07216878364870322f;
#define K_ITER(it_, RS, nit_)                                                                      \
  do {                                                                                             \
    float mine = 0.f;                                                                              \
    _Pragma("unroll") for (int j = 0; j < RD; ++j) {                                               \
      float d0 = 0.f, d1 = 0.f;                                                                    \
      _Pragma("unroll") for (int i = 0; i < 3; ++i) {                                              \
        d0 = dot2(ring[j][i][0], qp[i][0], d0); d1 = dot2(ring[j][i][1], qp[i][1], d1);            \
        d0 = dot2(ring[j][i][2], qp[i][2], d0); d1 = dot2(ring[j][i][3], qp[i][3], d1);            \
        ring[j][i] = __builtin_amdgcn_raw_buffer_load_b128(RS, voff + i * 128, SLOT_OFF(nit_, j), NT); \
        SB();                                                                                      \
      }                                                                                            \
      float d = d0 + d1;                                                                           \
      d += dpp<XOR1>(d); d += dpp<XOR2>(d); d += dpp<XOR7>(d);                                     \
      mine = ((seg & (RD - 1)) == j) ? d : mine;                                                   \
    }                                                                                              \
    sc[(it_) * RD + (seg & (RD - 1))] = rb(rb(mine) * scale);                                      \
  } while (0)
  for (int it = 0; it < NIT - 1; ++it) {
    K_ITER(it, rk, it + 1);
    if (LOOP_BAR) __builtin_amdgcn_s_barrier();
  }
  {
    u32x4 c0 = __builtin_amdgcn_raw_buffer_load_b128(rw, lane * 32, WS_C * 4, SC1), c1 = __builtin_amdgcn_raw_buffer_load_b128(rw, lane * 32 + 16, WS_C * 4, SC1);
    SB();
    K_ITER(NIT - 1, rv, 0);
#define C_OK() __all(c0[0] == tag_lo && c0[1] == tag_hi && c0[2] == tag_lo && c0[3] == tag_hi && c1[0] == tag_lo && c1[1] == tag_hi && c1[2] == tag_lo && c1[3] == tag_hi)
    if (!C_OK()) {
      SPIN {
        REPOLL();
        c0 = __builtin_amdgcn_raw_buffer_load_b128(rw, lane * 32, WS_C * 4, SC1); c1 = __builtin_amdgcn_raw_buffer_load_b128(rw, lane * 32 + 16, WS_C * 4, SC1);
        if (C_OK()) break;
      }
    }
  }
  STAMP0(2);
  float Mwg;
  {
    f32x4 s0 = *(LAS f32x4*)(sc + seg * 8), s1 = *(LAS f32x4*)(sc + seg * 8 + 4);
    float m = fmaxf(fmaxf(fmaxf(s0[0], s0[1]), fmaxf(s0[2], s0[3])), fmaxf(fmaxf(s1[0], s1[1]), fmaxf(s1[2], s1[3])));
    m = wave_max(m);
    if (lane == 0) sm[L_ML + wave] = m;
    lds_barrier();
    const f32x4 m0 = *(LAS f32x4*)(sm + L_ML), m1 = *(LAS f32x4*)(sm + L_ML + 4);
    Mwg = fmaxf(fmaxf(fmaxf(m0[0], m0[1]), fmaxf(m0[2], m0[3])), fmaxf(fmaxf(m1[0], m1[1]), fmaxf(m1[2], m1[3])));
    float l = 0.f;
#pragma unroll
    for (int k = 0; k < 4; ++k) {
      s0[k] = __builtin_amdgcn_exp2f((s0[k] - Mwg) * L2E); s1[k] = __builtin_amdgcn_exp2f((s1[k] - Mwg) * L2E);
      l += s0[k] + s1[k];
    }
    *(LAS f32x4*)(sc + seg * 8) = s0; *(LAS f32x4*)(sc + seg * 8 + 4) = s1;
    l = wave_sum(l);
    if (lane == 0) sm[L_ML + 8 + wave] = l;
    lds_barrier();
    const f32x4 l0 = *(LAS f32x4*)(sm + L_ML + 8), l1 = *(LAS f32x4*)(sm + L_ML + 12);
    const float Lwg = ((l0[0] + l0[1]) + (l0[2] + l0[3])) + ((l1[0] + l1[1]) + (l1[2] + l1[3]));
    __builtin_amdgcn_raw_buffer_store_b64(u32x2{seal(Mwg), seal(Lwg)}, rw, tid == 0 ? 0 : OOB, (WS_A + b * 256 + DH) * 4, SC1);
    SB();
  }
  f32x2 acc[12];
#pragma unroll
  for (int k = 0; k < 12; ++k) acc[k] = f32x2{0.f, 0.f};
#define V_ITER(SITE, it_, REISSUE, EXTRA)                                                              \
  do {                                                                                             \
    float pv[RD];                                                                                  \
    _Pragma("unroll") for (int j = 0; j < RD; j += 4) {                                            \
      const f32x4 t = *(LAS f32x4*)(sc + (it_) * RD + j);                                          \
      pv[j] = t[0]; pv[j + 1] = t[1]; pv[j + 2] = t[2]; pv[j + 3] = t[3];                          \
    }                                                                                              \
    _Pragma("unroll") for (int j = 0; j < RD; ++j)                                                 \
      _Pragma("unroll") for (int i = 0; i < 3; ++i) {                                              \
        axpy8<SITE>(acc + i * 4, pv[j], ring[j][i]);                                                     \
        if (REISSUE) ring[j][i] = __builtin_amdgcn_raw_buffer_load_b128(rv, voff + i * 128, SLOT_OFF((it_) + 1, j), NT); \
        { const int step = j * 3 + i; (void)step; EXTRA; }                                         \
        SB();                                                                                      \
      }                                                                                            \
  } while (0)
  for (int it = 0; it < NIT - 3; ++it) {
    V_ITER(1, it, true, );
    if (LOOP_BAR) __builtin_amdgcn_s_barrier();
  }
  float cfac;
  {
    const unsigned vml = (((lane & 7) * 32) * 256 + ((lane >> 3) & 1)) * 4;
    unsigned mlv = __builtin_amdgcn_raw_buffer_load_b32(rw, vml, (WS_A + h * 256 + DH) * 4, SC1);
    unsigned tk = 0;
    if (wave == 0) asm volatile("s_mov_b64 exec, 1\n\tglobal_atomic_add %0, %1, %2, %3 sc0\n\ts_mov_b64 exec, -1" : "=&v"(tk) : "v"(0u), "v"(1u), "s"(p.ws + WS_T) : "memory");
    SB();
    V_ITER(2, NIT - 3, true, );
    asm volatile("s_waitcnt vmcnt(%1)" : "+v"(tk) : "n"(3 * RD));
    if (tid == 0) su[L_ML + 16] = tk;
    if (!none_empty(mlv)) {
      SPIN {
        REPOLL();
        mlv = __builtin_amdgcn_raw_buffer_load_b32(rw, vml, (WS_A + h * 256 + DH) * 4, SC1);
        if (none_empty(mlv)) break;
      }
    }
    const float ml = u2f(mlv);
    float M = ml;
    M = fmaxf(M, dpp<XOR1>(M)); M = fmaxf(M, dpp<XOR2>(M)); M = fmaxf(M, dpp<XOR7>(M));
    float L = __builtin_amdgcn_exp2f((ml - M) * L2E) * dpp<ROR8>(ml);
    L += dpp<XOR1>(L); L += dpp<XOR2>(L); L += dpp<XOR7>(L);
    cfac = __builtin_amdgcn_exp2f((Mwg - lane_val(M, 0)) * L2E) / lane_val(L, 0);
    PIN(cfac);
    lds_barrier();
  }
  const int mj = __builtin_amdgcn_readfirstlane(su[L_ML + 16]);
  const bool is_mlp = mj < NMLP;
  const bool up = lane & 8;
  const int k0 = (up ? 3 : 0) + ((lane & 16) ? 6 : 0) + ((lane & 32) ? 12 : 0);
  LAS float* red0 = sm + L_RED + wave * DH + seg * 8 + (k0 >> 3) * 64 + (k0 & 7);
  LAS float* red1 = sm + L_RED + wave * DH + seg * 8 + ((k0 + 1) >> 3) * 64 + ((k0 + 1) & 7);
  LAS float* red2 = sm + L_RED + wave * DH + seg * 8 + ((k0 + 2) >> 3) * 64 + ((k0 + 2) & 7);
  PIN(red0); PIN(red1); PIN(red2);
#define WAVE_PARTIAL()                                                                             \
  do {                                                                                             \
    float a_[12], c_[6];                                                                           \
    _Pragma("unroll") for (int k = 0; k < 12; ++k) a_[k] = tr32(acc[k >> 1][k & 1], acc[(k + 12) >> 1][k & 1]); \
    _Pragma("unroll") for (int k = 0; k < 6; ++k) c_[k] = tr16(a_[k], a_[k + 6]);                  \
    *red0 = (up ? c_[3] : c_[0]) + dpp<ROR8>(up ? c_[0] : c_[3]);                                  \
    *red1 = (up ? c_[4] : c_[1]) + dpp<ROR8>(up ? c_[1] : c_[4]);                                  \
    *red2 = (up ? c_[5] : c_[2]) + dpp<ROR8>(up ? c_[2] : c_[5]);                                  \
    lds_barrier();                                                                                 \
  } while (0)

  if (wave < 3) {
    V_ITER(3, NIT - 2, true, );
    __builtin_amdgcn_s_barrier();
    V_ITER(4, NIT - 1, false, );
    STAMP0(3);
    WAVE_PARTIAL();
    float t[NW];
#pragma unroll
    for (int w = 0; w < NW; ++w) t[w] = sm[L_RED + w * DH + tid];
    const float part = (((t[0] + t[1]) + (t[2] + t[3])) + ((t[4] + t[5]) + (t[6] + t[7]))) * cfac;
    __builtin_amdgcn_raw_buffer_store_b32(seal(part), rw, tid * 4, (WS_A + b * 256) * 4, SC1);
    STAMP0(4);
    unsigned v[NSPLIT];
    SPIN {
#pragma unroll
      for (int j = 0; j < NSPLIT; ++j) v[j] = __builtin_amdgcn_raw_buffer_load_b32(rw, tid * 4, (WS_A + (h + 32 * j) * 256) * 4, SC1);
      if (none_empty(umax(umax(umax(v[0], v[1]), umax(v[2], v[3])), umax(umax(v[4], v[5]), umax(v[6], v[7]))))) break;
      REPOLL();
    }
    const float o = ((u2f(v[0]) + u2f(v[1])) + (u2f(v[2]) + u2f(v[3]))) + ((u2f(v[4]) + u2f(v[5])) + (u2f(v[6]) + u2f(v[7])));
    ((LAS __bf16*)(su + L_O))[tid] = (__bf16)o;
    lds_barrier();
    if (is_mlp) { __builtin_amdgcn_s_barrier(); __builtin_amdgcn_s_barrier(); }
    __builtin_amdgcn_endpgm();
  }

  const int g = wave - 3;
  LAS unsigned* stage = su + L_W + g * 2048;
  const auto ro = __builtin_amdgcn_make_buffer_rsrc((void*)p.wo, 0, HD * D * 2, 0x00020000);
#define LAST_AND_WO(SITE)                                                                          \
  do {                                                                                             \
    const unsigned vo = (unsigned)((lane >> 1) * (D * 2) + (lane & 1) * 16);                       \
    u32x4 wor[6];                                                                                  \
    if (WO_FIRST) {                                                                                \
      _Pragma("unroll") for (int i = 0; i < 6; ++i) { wor[i] = __builtin_amdgcn_raw_buffer_load_b128(ro, vo, (h * DH + i * 32) * (D * 2) + s * 160 + g * 32, W_AUX); SB(); } \
    }                                                                                              \
    V_ITER(SITE, NIT - 1, false,                                                                   \
      if (!WO_FIRST && step < 6) wor[step] = __builtin_amdgcn_raw_buffer_load_b128(ro, vo, (h * DH + step * 32) * (D * 2) + s * 160 + g * 32, W_AUX)); \
    WAVE_PARTIAL();                                                                                \
    bf16x8 wof[6], of[6];                                                                          \
    _Pragma("unroll") for (int i = 0; i < 6; ++i) *(LAS u32x4*)(stage + (i * 64 + lane) * 4) = wor[i]; \
    _Pragma("unroll") for (int t = 0; t < 6; ++t) wof[t] = frag_tr(stage, lane, t);                \
    unsigned vyb = (lane & 15) == 0 ? lane : OOB;                                                  \
    PIN(vyb);                                                                                      \
    lds_barrier();                                                                                 \
    STAMP3(5);                                                                                     \
    _Pragma("unroll") for (int t = 0; t < 6; ++t) of[t] = frag_vec(su + L_O, lane, t);             \
    SB();                                                                                          \
    f32x4 y0 = {0.f, 0.f, 0.f, 0.f}, y1 = {0.f, 0.f, 0.f, 0.f};                                    \
    _Pragma("unroll") for (int t = 0; t < 6; t += 2) {     \
      y0 = __builtin_amdgcn_mfma_f32_16x16x32_bf16(wof[t], of[t], y0, 0, 0, 0);                    \
      y1 = __builtin_amdgcn_mfma_f32_16x16x32_bf16(wof[t + 1], of[t + 1], y1, 0, 0, 0);            \
    }                                                                                              \
    y0 += y1;                                                                                      \
    __builtin_amdgcn_raw_buffer_store_b128(u32x4{seal(y0[0]), seal(y0[1]), seal(y0[2]), seal(y0[3])}, rw, vyb, (WS_B + h * D + s * 80 + g * 16) * 4, SC1); \
    STAMP3(6);                                                                                     \
  } while (0)

  if (!is_mlp) {
    V_ITER(5, NIT - 2, true, );
    __builtin_amdgcn_s_barrier();
    LAST_AND_WO(6);
    __builtin_amdgcn_endpgm();
  }

  u32x4 wgr[4], wur[4];
  unsigned wdr[16];
  const unsigned xp = ((const unsigned*)p.x)[g * 64 + 2 * (lane & 31) + (lane >> 5)];
  {
    const auto rg_ = __builtin_amdgcn_make_buffer_rsrc((void*)p.wg, 0, D * F * 2, 0x00020000);
    const auto ru_ = __builtin_amdgcn_make_buffer_rsrc((void*)p.wu, 0, D * F * 2, 0x00020000);
    const auto rd_ = __builtin_amdgcn_make_buffer_rsrc((void*)p.wd, 0, D * F * 2, 0x00020000);
    const unsigned vg = (unsigned)((lane >> 1) * (F * 2) + (lane & 1) * 16);
    V_ITER(7, NIT - 2, true,
      if (step < 4) {
        wgr[step] = __builtin_amdgcn_raw_buffer_load_b128(rg_, vg, (g * 128 + step * 32) * (F * 2) + mj * (FPW * 2), W_AUX);
        wur[step] = __builtin_amdgcn_raw_buffer_load_b128(ru_, vg, (g * 128 + step * 32) * (F * 2) + mj * (FPW * 2), W_AUX);
      } else if (step < 12) {
        wdr[2 * step - 8] = __builtin_amdgcn_raw_buffer_load_b32(rd_, lane * 4, (mj * FPW + 2 * step - 8) * (D * 2) + g * 256, W_AUX);
        wdr[2 * step - 7] = __builtin_amdgcn_raw_buffer_load_b32(rd_, lane * 4, (mj * FPW + 2 * step - 7) * (D * 2) + g * 256, W_AUX);
      });
  }
  __builtin_amdgcn_s_barrier();
  LAST_AND_WO(8);
  bf16x8 wgf[4], wuf[4];
  unsigned wd0[8], wd1[8], hp;
#pragma unroll
  for (int i = 0; i < 4; ++i) { *(LAS u32x4*)(stage + (i * 64 + lane) * 4) = wgr[i]; *(LAS u32x4*)(stage + 1024 + (i * 64 + lane) * 4) = wur[i]; }
#pragma unroll
  for (int t = 0; t < 4; ++t) { wgf[t] = frag_tr(stage, lane, t); wuf[t] = frag_tr(stage + 1024, lane, t); }
#pragma unroll
  for (int i = 0; i < 8; ++i) {
    wd0[i] = __builtin_amdgcn_perm(wdr[2 * i + 1], wdr[2 * i], 0x05040100u); wd1[i] = __builtin_amdgcn_perm(wdr[2 * i + 1], wdr[2 * i], 0x07060302u);
    PIN(wd0[i]); PIN(wd1[i]);
  }
  float xlo = bflo(xp), xhi = bfhi(xp);
  LAS unsigned* hslot = su + g * 64 + 2 * (lane & 31) + (lane >> 5);
  PIN(xlo); PIN(xhi); PIN(hslot);
  {
    f32x4 S[4];
    unsigned need = 15;
    const unsigned vb = (unsigned)((lane >> 5) * D + g * 128 + (lane & 31) * 4) * 4;
    SPIN {
      u32x4 v[4][4];
#pragma unroll
      for (int q = 0; q < 4; ++q)
        if (need & (1u << q)) {
#pragma unroll
          for (int j = 0; j < 4; ++j) v[q][j] = __builtin_amdgcn_raw_buffer_load_b128(rw, vb, (WS_B + (2 * q + 8 * j) * D) * 4, SC1);
        }
#pragma unroll
      for (int q = 0; q < 4; ++q)
        if (need & (1u << q)) {
          if (none_empty(umax(umax(umax4(v[q][0]), umax4(v[q][1])), umax(umax4(v[q][2]), umax4(v[q][3]))))) {
            S[q] = (__builtin_bit_cast(f32x4, v[q][0]) + __builtin_bit_cast(f32x4, v[q][1])) + (__builtin_bit_cast(f32x4, v[q][2]) + __builtin_bit_cast(f32x4, v[q][3]));
            need &= ~(1u << q);
          }
        }
      if (!need) break;
      REPOLL();
    }
    STAMP3(7);
    const f32x4 y = (S[0] + S[1]) + (S[2] + S[3]);
    const float ya = tr32(y[0], y[2]), yb = tr32(y[1], y[3]);
    const unsigned yp = pack_bf(ya, yb);
    hp = pack_bf(xlo + bflo(yp), xhi + bfhi(yp));
    hslot[L_H] = hp;
  }
  lds_barrier();
  {
    unsigned hr[5];
#pragma unroll
    for (int j = 0; j < 5; ++j) hr[j] = su[L_H + j * 64 + lane];
    float m, rinv;
    ln_stats(hr, m, rinv);
    const unsigned dp = pack_bf(bflo(hp) - m, bfhi(hp) - m);
    hslot[L_XN] = pack_bf(bflo(dp) * rinv, bfhi(dp) * rinv);
    bf16x8 xf[4];
#pragma unroll
    for (int t = 0; t < 4; ++t) xf[t] = frag_vec(su + L_XN + g * 64, lane, t);
    SB();
    f32x4 ga = {0.f, 0.f, 0.f, 0.f}, ua = {0.f, 0.f, 0.f, 0.f};
#pragma unroll
    for (int t = 0; t < 4; ++t) {
      ga = __builtin_amdgcn_mfma_f32_16x16x32_bf16(xf[t], wgf[t], ga, 0, 0, 0);
      ua = __builtin_amdgcn_mfma_f32_16x16x32_bf16(xf[t], wuf[t], ua, 0, 0, 0);
    }
    sm[L_GU + g * 32 + (lane & 31)] = (lane & 16) ? ua[0] : ga[0];
  }
  lds_barrier();
  {
    const int n = lane & 15;
    float gs[5], us[5];
#pragma unroll
    for (int w = 0; w < 5; ++w) { gs[w] = sm[L_GU + w * 32 + n]; us[w] = sm[L_GU + w * 32 + 16 + n]; }
    const float gt = rb(((gs[0] + gs[1]) + (gs[2] + gs[3])) + gs[4]), ut = rb(((us[0] + us[1]) + (us[2] + us[3])) + us[4]);
    const float act = rb(__builtin_amdgcn_rcpf(1.f + __builtin_amdgcn_exp2f(-L2E * gt))) * ut;
    const unsigned ap = pack_bf(act, dpp<XOR1>(act));
    unsigned a2[8];
#pragma unroll
    for (int i = 0; i < 8; ++i) a2[i] = __builtin_amdgcn_readlane(ap, 2 * i);
    SB();
    float d0 = 0.f, d1 = 0.f;
#pragma unroll
    for (int i = 0; i < 8; ++i) { d0 = dot2(wd0[i], a2[i], d0); d1 = dot2(wd1[i], a2[i], d1); }
    const unsigned e0 = seal(d0), e1 = seal(d1);
    __builtin_amdgcn_raw_buffer_store_b128(u32x4{e0, e1, f2u(dpp<XOR1>(u2f(e0))), f2u(dpp<XOR1>(u2f(e1)))}, rw, (lane & 1) ? OOB : lane * 8, (WS_D + mj * D + g * 128) * 4, SC1);
  }
  STAMP3(8);
  if (mj >= NFIN || wave != 3) return;
  {
    const int col = (lane & 3) * 4 + ((lane >> 4) & 1) + 2 * (lane >> 5);
    const unsigned hw = su[L_H + mj * 8 + (col >> 1)];
    float hv = (col & 1) ? bfhi(hw) : bflo(hw);
    unsigned short* op = p.out + mj * 16 + col;
    PIN(hv); PIN(op);
    const unsigned vd = (unsigned)((lane >> 2) * D + (lane & 3) * 4) * 4;
    u32x4 v[6];
    SPIN {
#pragma unroll
      for (int i = 0; i < 6; ++i) v[i] = __builtin_amdgcn_raw_buffer_load_b128(rw, vd, (WS_D + i * 16 * D + mj * 16) * 4, SC1);
      if (none_empty(umax(umax(umax(umax4(v[0]), umax4(v[1])), umax(umax4(v[2]), umax4(v[3]))), umax(umax4(v[4]), umax4(v[5]))))) break;
      REPOLL();
    }
    const f32x4 t = ((__builtin_bit_cast(f32x4, v[0]) + __builtin_bit_cast(f32x4, v[1])) + (__builtin_bit_cast(f32x4, v[2]) + __builtin_bit_cast(f32x4, v[3]))) +
                    (__builtin_bit_cast(f32x4, v[4]) + __builtin_bit_cast(f32x4, v[5]));
    float e = tr16(tr32(t[0], t[2]), tr32(t[1], t[3]));
    e += dpp<ROR8>(e); e += dpp<ROR4>(e);
    const __bf16 o = (__bf16)(hv + rb(e));
    if ((lane & 12) == 0) *(GAS unsigned short*)op = __builtin_bit_cast(unsigned short, o);
    if (mj == 0) __builtin_amdgcn_raw_buffer_store_b64(u32x2{tag_lo, tag_hi}, rw, lane == 0 ? 0 : OOB, WS_E * 4, SC1);
  }
  STAMP3(9);
}

}

extern "C" void kernel_launch(void* const* d_in, const int* in_sizes, int n_in, void* d_out, int out_size, void* d_ws, size_t ws_size,
                              hipStream_t stream) {
  Params p;
  p.x = (const unsigned short*)d_in[0];
  p.kc = (const unsigned short*)d_in[1];
  p.vc = (const unsigned short*)d_in[2];
  p.wq = (const unsigned short*)d_in[3];
  p.wo = (const unsigned short*)d_in[4];
  p.wg = (const unsigned short*)d_in[6];
  p.wu = (const unsigned short*)d_in[7];
  p.wd = (const unsigned short*)d_in[8];
  p.out = (unsigned short*)d_out;
  p.ws = (unsigned*)d_ws;
  (void)hipFuncSetAttribute((const void*)fused_decode, hipFuncAttributeMaxDynamicSharedMemorySize, LDS_BYTES);
  fused_decode<<<NWG, NTH, LDS_BYTES, stream>>>(p);
}
```

```cpp
#include <hip/hip_runtime.h>
#include <hip/hip_bf16.h>
#include <stdint.h>

#define DIAG 0
#define UT 4
#define RC 1
#define B1 2
#define QSLEEP 16
#define DYN_ROLE 1
#define WQ_AUX 0
#define W_AUX 0
#define K_AUX 2
#define V_AUX 2
#define WO_FIRST 1
#define WIDE_D 1
#define LOOP_BAR 1

namespace {

constexpr int D = 640, H = 32, SK = 32768, DH = 192, F = 1536, HD = H * DH;
constexpr int NWG = 256, NW = 8, NTH = NW * 64, NSPLIT = 8;
constexpr int U = UT;
constexpr int RD = UT * RC;
constexpr int TPW = SK / 8 / (NSPLIT * NW);
constexpr int NIT = TPW / RD;
constexpr unsigned TILE_B = 8 * DH * 2;
constexpr unsigned KV_BYTES = (unsigned)H * SK * DH * 2;
constexpr unsigned CH_STRIDE = NSPLIT * NW * U * TILE_B;
constexpr int NMLP = 96, FPW = F / NMLP;
constexpr int NFIN = 40;

constexpr int WS_E = 0;
constexpr int WS_T = 64;
constexpr int WS_C = 128;
constexpr int WS_Q = 1024;
constexpr int WS_A = WS_Q + NWG * 2 * DH;
constexpr int WS_B = WS_A + NWG * 256;
constexpr int WS_D = WS_B + H * D;
constexpr int WS_END = WS_D + NMLP * D;
constexpr int WS_DIAG = 1 << 20;
constexpr unsigned OOB = 0x80000000u;
constexpr unsigned EMPTY = 0xFFFFFFFFu;
constexpr unsigned SPIN_CAP = 1u << 20;

constexpr int SC_STR = 68;
constexpr int L_SC = 0;
constexpr int L_RED = L_SC + NW * 8 * SC_STR;
constexpr int L_Q = L_RED + 16 * DH;
constexpr int L_ML = L_Q + 96;
constexpr int L_O = L_ML + 20;
constexpr int L_H = L_O + 96;
constexpr int L_XN = L_H + 320;
constexpr int L_GU = L_XN + 320;
constexpr int L_W = L_GU + 160;
constexpr int L_END = L_W + 5 * 2048;
constexpr int LDS_BYTES = 96 * 1024;
static_assert(L_END * 4 <= LDS_BYTES && L_W % 4 == 0 && L_O % 4 == 0 && L_XN % 4 == 0 && L_Q % 4 == 0, "lds");

typedef unsigned u32x4 __attribute__((ext_vector_type(4)));
typedef unsigned u32x2 __attribute__((ext_vector_type(2)));
typedef float f32x4 __attribute__((ext_vector_type(4)));
typedef float f32x2 __attribute__((ext_vector_type(2)));
typedef __bf16 bf16x2 __attribute__((ext_vector_type(2)));
typedef __bf16 bf16x8 __attribute__((ext_vector_type(8)));
typedef short s16x4 __attribute__((ext_vector_type(4)));
typedef short s16x8 __attribute__((ext_vector_type(8)));
#define LAS __attribute__((address_space(3)))
#define GAS __attribute__((address_space(1)))
#define CAS __attribute__((address_space(4)))
#define RLX_AGENT __ATOMIC_RELAXED, __HIP_MEMORY_SCOPE_AGENT
#define SB() __builtin_amdgcn_sched_barrier(0)
#define PIN(x) asm volatile("" : "+v"(x))
#define REPOLL() asm volatile("s_sleep 1" ::: "memory")
#define SPIN _Pragma("nounroll") for (unsigned spins = 0; spins < SPIN_CAP; ++spins)
constexpr int SC1 = 16, NT = 2;
constexpr float L2E = 1.4426950408889634f;

__device__ __forceinline__ float u2f(unsigned u) { return __builtin_bit_cast(float, u); }
__device__ __forceinline__ unsigned f2u(float f) { return __builtin_bit_cast(unsigned, f); }
__device__ __forceinline__ float bflo(unsigned u) { return u2f(u << 16); }
__device__ __forceinline__ float bfhi(unsigned u) { return u2f(u & 0xffff0000u); }
__device__ __forceinline__ float rb(float x) { return (float)(__bf16)x; }
__device__ __forceinline__ unsigned pack_bf(float lo, float hi) {
  bf16x2 v = {(__bf16)lo, (__bf16)hi};
  return __builtin_bit_cast(unsigned, v);
}
__device__ __forceinline__ float dot2(unsigned k, unsigned q, float acc) {
  return __builtin_amdgcn_fdot2_f32_bf16(__builtin_bit_cast(bf16x2, k), __builtin_bit_cast(bf16x2, q), acc, false);
}
template <int CTRL> __device__ __forceinline__ float dpp(float x) {
  return u2f(__builtin_amdgcn_mov_dpp(f2u(x), CTRL, 0xf, 0xf, true));
}
constexpr int XOR1 = 0xB1, XOR2 = 0x4E, XOR7 = 0x141, ROR8 = 0x128, ROR4 = 0x124;
__device__ __forceinline__ float lane_val(float v, int l) { return u2f(__builtin_amdgcn_readlane(f2u(v), l)); }
__device__ __forceinline__ float tr32(float a, float b) {
  auto r = __builtin_amdgcn_permlane32_swap(f2u(a), f2u(b), false, false);
  return u2f(r[0]) + u2f(r[1]);
}
__device__ __forceinline__ float tr16(float a, float b) {
  auto r = __builtin_amdgcn_permlane16_swap(f2u(a), f2u(b), false, false);
  return u2f(r[0]) + u2f(r[1]);
}
__device__ __forceinline__ float wave_sum(float v) {
  v += dpp<XOR1>(v); v += dpp<XOR2>(v); v += dpp<XOR7>(v); v += dpp<ROR8>(v);
  v += u2f(__builtin_amdgcn_update_dpp(0, f2u(v), 0x142, 0xa, 0xf, false));
  v += u2f(__builtin_amdgcn_update_dpp(0, f2u(v), 0x143, 0xc, 0xf, false));
  return lane_val(v, 63);
}
__device__ __forceinline__ float wave_max(float v) {
  v = fmaxf(v, dpp<XOR1>(v)); v = fmaxf(v, dpp<XOR2>(v)); v = fmaxf(v, dpp<XOR7>(v)); v = fmaxf(v, dpp<ROR8>(v));
  auto r = __builtin_amdgcn_permlane16_swap(f2u(v), f2u(v), false, false);
  v = fmaxf(u2f(r[0]), u2f(r[1]));
  auto t = __builtin_amdgcn_permlane32_swap(f2u(v), f2u(v), false, false);
  return fmaxf(u2f(t[0]), u2f(t[1]));
}
__device__ __forceinline__ void lds_barrier() {
  asm volatile("s_waitcnt lgkmcnt(0)" ::: "memory");
  __builtin_amdgcn_s_barrier();
  asm volatile("" ::: "memory");
}
__device__ __forceinline__ unsigned umax(unsigned a, unsigned b) { return a > b ? a : b; }
__device__ __forceinline__ unsigned umax4(u32x4 v) { return umax(umax(v[0], v[1]), umax(v[2], v[3])); }
__device__ __forceinline__ unsigned seal(float v) { const unsigned u = f2u(v); return u < EMPTY - 1 ? u : EMPTY - 1; }
__device__ __forceinline__ bool none_empty(unsigned mx) { return __builtin_amdgcn_ballot_w64(mx == EMPTY) == 0; }

template <int SITE> __device__ __forceinline__ void axpy8(f32x2* acc, float pv, u32x4 w) {
  asm volatile("; site %1" : "+v"(w) : "n"(SITE));
  const f32x2 pp = {pv, pv};
#pragma unroll
  for (int k = 0; k < 4; ++k) acc[k] = __builtin_elementwise_fma(pp, f32x2{bflo(w[k]), bfhi(w[k])}, acc[k]);
  asm volatile("" : "+v"(acc[0]), "+v"(acc[1]), "+v"(acc[2]), "+v"(acc[3]));
}

__device__ __forceinline__ void ln_stats(const unsigned (&pr)[5], float& m, float& rinv) {
  float v[10], s = 0.f;
#pragma unroll
  for (int j = 0; j < 5; ++j) { v[2 * j] = bflo(pr[j]); v[2 * j + 1] = bfhi(pr[j]); }
  s = ((v[0] + v[1]) + (v[2] + v[3])) + ((v[4] + v[5]) + (v[6] + v[7])) + (v[8] + v[9]);
  const float mean = wave_sum(s) * (1.f / D);
  float q0 = 0.f, q1 = 0.f;
#pragma unroll
  for (int j = 0; j < 5; ++j) { const float d0 = v[2 * j] - mean, d1 = v[2 * j + 1] - mean; q0 = fmaf(d0, d0, q0); q1 = fmaf(d1, d1, q1); }
  const float var = rb(wave_sum(q0 + q1) * (1.f / D));
  m = rb(mean);
  rinv = __builtin_amdgcn_rcpf(rb(__builtin_amdgcn_sqrtf(rb(var + 1e-5f))));
}

__device__ __forceinline__ bf16x8 frag_tr(LAS unsigned* img, int lane, int t) {
  LAS unsigned char* a = (LAS unsigned char*)img + (32 * t + 8 * (lane >> 4) + ((lane >> 2) & 3)) * 32 + (lane & 3) * 8;
  const s16x4 lo = __builtin_amdgcn_ds_read_tr16_b64_v4i16((LAS s16x4*)a);
  const s16x4 hi = __builtin_amdgcn_ds_read_tr16_b64_v4i16((LAS s16x4*)(a + 128));
  return __builtin_bit_cast(bf16x8, __builtin_shufflevector(lo, hi, 0, 1, 2, 3, 4, 5, 6, 7));
}
__device__ __forceinline__ bf16x8 frag_vec(LAS unsigned* vec, int lane, int t) {
  return __builtin_bit_cast(bf16x8, *(LAS u32x4*)(vec + 16 * t + 4 * (lane >> 4)));
}

struct Params {
  const unsigned short *x, *kc, *vc, *wq, *wo, *wg, *wu, *wd;
  unsigned short* out;
  unsigned* ws;
};

#define STAMP(i) asm volatile("; MARK " #i)
#define STAMP0(i) do { if (wave == 0) STAMP(i); } while (0)
#define STAMP3(i) do { if (wave == 3) STAMP(i); } while (0)

__global__ void __launch_bounds__(NTH, 2) fused_decode(Params p) {
  extern __shared__ __attribute__((aligned(16))) float smem_[];
  LAS float* sm = (LAS float*)smem_;
  LAS unsigned* su = (LAS unsigned*)smem_;
  const int tid = threadIdx.x, lane = tid & 63, wave = __builtin_amdgcn_readfirstlane(tid >> 6);
  const int b = blockIdx.x, h = b & 31, s = b >> 5;
  const int r = lane >> 3, seg = lane & 7;
  STAMP0(0);

  const auto rk = __builtin_amdgcn_make_buffer_rsrc((void*)p.kc, 0, (int)KV_BYTES, 0x00020000);
  const auto rv = __builtin_amdgcn_make_buffer_rsrc((void*)p.vc, 0, (int)KV_BYTES, 0x00020000);
  const auto rq = __builtin_amdgcn_make_buffer_rsrc((void*)p.wq, 0, D * HD * 2, 0x00020000);
  const auto rw = __builtin_amdgcn_make_buffer_rsrc((void*)p.ws, 0, WS_END * 4, 0x00020000);
  const unsigned voff = r * (DH * 2) + seg * 16;
  const unsigned so0 = (unsigned)h * (SK * DH * 2) + (s * NW + wave) * (U * TILE_B);
#define SLOT_OFF(it_, j_) (so0 + ((it_) * RC + (j_) / U) * CH_STRIDE + ((j_) % U) * TILE_B)

  unsigned xr[5];
#pragma unroll
  for (int j = 0; j < 5; ++j) xr[j] = ((const unsigned*)p.x)[j * 64 + lane];
  SB();
  const int qrr = lane >= 24, qsg = lane - 24 * qrr;
  u32x4 wqr[5];
#pragma unroll
  for (int j = 0; j < 5; ++j) {
    wqr[j] = __builtin_amdgcn_raw_buffer_load_b128(rq, lane < 48 ? qrr * (HD * 2) + qsg * 16 : OOB, (s * 80 + wave * 10 + 2 * j) * (HD * 2) + h * (DH * 2), WQ_AUX);
    SB();
  }
  const unsigned long long ep = __hip_atomic_load((GAS unsigned long long*)(p.ws + WS_E), RLX_AGENT);
  SB();
  if (wave == NW - 1) {
    const u32x4 e4 = {EMPTY, EMPTY, EMPTY, EMPTY};
    __builtin_amdgcn_raw_buffer_store_b128(e4, rw, lane < 49 ? lane * 16 : OOB, (WS_A + b * 256) * 4, SC1);
    __builtin_amdgcn_raw_buffer_store_b128(e4, rw, lane < 20 ? lane * 16 : OOB, (WS_B + h * D + s * 80) * 4, SC1);
    if (b < NMLP) {
#pragma unroll
      for (int j = 0; j < 3; ++j) __builtin_amdgcn_raw_buffer_store_b128(e4, rw, j * 64 + lane < 160 ? (j * 64 + lane) * 16 : OOB, (WS_D + b * D) * 4, SC1);
    }
    if (b == 0) __builtin_amdgcn_raw_buffer_store_b32(0u, rw, lane == 0 ? 0 : OOB, WS_T * 4, SC1);
  }
  SB();
  __builtin_amdgcn_s_barrier();
  u32x4 ring[RD][3];
#pragma unroll
  for (int j = 0; j < B1; ++j)
#pragma unroll
    for (int i = 0; i < 3; ++i) {
      ring[j][i] = __builtin_amdgcn_raw_buffer_load_b128(rk, voff + i * 128, SLOT_OFF(0, j), K_AUX);
      SB();
    }
  {
    const CAS unsigned* xs = (const CAS unsigned*)p.x + s * 40 + wave * 5;
    float m, rinv;
    ln_stats(xr, m, rinv);
    f32x2 a4[4];
#pragma unroll
    for (int k = 0; k < 4; ++k) a4[k] = f32x2{0.f, 0.f};
#pragma unroll
    for (int j = 0; j < 5; ++j) {
      const unsigned xp = xs[j];
      const float xv = qrr ? bfhi(xp) : bflo(xp);
      axpy8<0>(a4, rb(rb(xv - m) * rinv), wqr[j]);
    }
    if (lane < 48) {
      LAS float* dst = sm + L_RED + (wave * 2 + qrr) * DH + qsg * 8;
      *(LAS f32x4*)dst = f32x4{a4[0][0], a4[0][1], a4[1][0], a4[1][1]};
      *(LAS f32x4*)(dst + 4) = f32x4{a4[2][0], a4[2][1], a4[3][0], a4[3][1]};
    }
  }
  const unsigned tag_lo = __builtin_amdgcn_readfirstlane((unsigned)(ep + 1)), tag_hi = __builtin_amdgcn_readfirstlane((unsigned)((ep + 1) >> 32));
  lds_barrier();
  STAMP0(10);
  if (wave < 3) {
    float t0 = 0.f, t1 = 0.f;
#pragma unroll
    for (int w = 0; w < 16; w += 2) { t0 += sm[L_RED + w * DH + tid]; t1 += sm[L_RED + (w + 1) * DH + tid]; }
    __builtin_amdgcn_raw_buffer_store_b64(u32x2{f2u(t0 + t1), tag_lo}, rw, tid * 8, WS_Q * 4 + b * (DH * 8), SC1);
  }
  if (wave == NW - 1) {
    asm volatile("s_waitcnt vmcnt(%0)" ::"n"(3 * B1) : "memory");
    __builtin_amdgcn_raw_buffer_store_b64(u32x2{tag_lo, tag_hi}, rw, lane == 0 ? 0 : OOB, WS_C * 4 + b * 8, SC1);
  }
  __builtin_amdgcn_s_sleep(QSLEEP);
  {
    u32x2 g[NSPLIT];
    const unsigned vq = wave < 3 ? (unsigned)tid * 8 : OOB;
#pragma unroll
    for (int j = 0; j < NSPLIT; ++j) { g[j] = __builtin_amdgcn_raw_buffer_load_b64(rw, vq, WS_Q * 4 + (h + 32 * j) * (DH * 8), SC1); SB(); }
    __builtin_amdgcn_s_barrier();
#pragma unroll
    for (int j = B1; j < RD; ++j)
#pragma unroll
      for (int i = 0; i < 3; ++i) {
        ring[j][i] = __builtin_amdgcn_raw_buffer_load_b128(rk, voff + i * 128, SLOT_OFF(0, j), K_AUX);
        SB();
      }
    if (wave < 3) {
      bool ok = true;
#pragma unroll
      for (int j = 0; j < NSPLIT; ++j) ok &= g[j][1] == tag_lo;
      if (!__all(ok)) {
        STAMP0(11);
        SPIN {
          REPOLL();
          ok = true;
#pragma unroll
          for (int j = 0; j < NSPLIT; ++j) { g[j] = __builtin_amdgcn_raw_buffer_load_b64(rw, vq, WS_Q * 4 + (h + 32 * j) * (DH * 8), SC1); ok &= g[j][1] == tag_lo; }
          if (__all(ok)) break;
        }
      }
      const float t = ((u2f(g[0][0]) + u2f(g[1][0])) + (u2f(g[2][0]) + u2f(g[3][0]))) + ((u2f(g[4][0]) + u2f(g[5][0])) + (u2f(g[6][0]) + u2f(g[7][0])));
      const float tn = dpp<XOR1>(t);
      if (!(lane & 1)) su[L_Q + (tid >> 1)] = pack_bf(t, tn);
    }
  }
  lds_barrier();
  u32x4 qp[3];
#pragma unroll
  for (int i = 0; i < 3; ++i) qp[i] = *(LAS u32x4*)(su + L_Q + i * 32 + seg * 4);
  STAMP0(1);

  LAS float* sc = sm + L_SC + (wave * 8 + r) * SC_STR;
  const float scale = 0.07216878364870322f;
#define K_ITER(it_, RS, nit_, AUX_)                                                                    \
  do {                                                                                             \
    float mine = 0.f;                                                                              \
    _Pragma("unroll") for (int j = 0; j < RD; ++j) {                                               \
      float d0 = 0.f, d1 = 0.f;                                                                    \
      _Pragma("unroll") for (int i = 0; i < 3; ++i) {                                              \
        d0 = dot2(ring[j][i][0], qp[i][0], d0); d1 = dot2(ring[j][i][1], qp[i][1], d1);            \
        d0 = dot2(ring[j][i][2], qp[i][2], d0); d1 = dot2(ring[j][i][3], qp[i][3], d1);            \
        ring[j][i] = __builtin_amdgcn_raw_buffer_load_b128(RS, voff + i * 128, SLOT_OFF(nit_, j), AUX_); \
        SB();                                                                                      \
      }                                                                                            \
      float d = d0 + d1;                                                                           \
      d += dpp<XOR1>(d); d += dpp<XOR2>(d); d += dpp<XOR7>(d);                                     \
      mine = ((seg & (RD - 1)) == j) ? d : mine;                                                   \
    }                                                                                              \
    sc[(it_) * RD + (seg & (RD - 1))] = rb(rb(mine) * scale);                                      \
  } while (0)
  for (int it = 0; it < NIT - 1; ++it) {
    K_ITER(it, rk, it + 1, K_AUX);
    if (LOOP_BAR) __builtin_amdgcn_s_barrier();
  }
  {
    u32x4 c0 = __builtin_amdgcn_raw_buffer_load_b128(rw, lane * 32, WS_C * 4, SC1), c1 = __builtin_amdgcn_raw_buffer_load_b128(rw, lane * 32 + 16, WS_C * 4, SC1);
    SB();
    K_ITER(NIT - 1, rv, 0, V_AUX);
#define C_OK() __all(c0[0] == tag_lo && c0[1] == tag_hi && c0[2] == tag_lo && c0[3] == tag_hi && c1[0] == tag_lo && c1[1] == tag_hi && c1[2] == tag_lo && c1[3] == tag_hi)
    if (!C_OK()) {
      SPIN {
        REPOLL();
        c0 = __builtin_amdgcn_raw_buffer_load_b128(rw, lane * 32, WS_C * 4, SC1); c1 = __builtin_amdgcn_raw_buffer_load_b128(rw, lane * 32 + 16, WS_C * 4, SC1);
        if (C_OK()) break;
      }
    }
  }
  STAMP0(2);
  float Mwg;
  {
    f32x4 s0 = *(LAS f32x4*)(sc + seg * 8), s1 = *(LAS f32x4*)(sc + seg * 8 + 4);
    float m = fmaxf(fmaxf(fmaxf(s0[0], s0[1]), fmaxf(s0[2], s0[3])), fmaxf(fmaxf(s1[0], s1[1]), fmaxf(s1[2], s1[3])));
    m = wave_max(m);
    if (lane == 0) sm[L_ML + wave] = m;
    lds_barrier();
    const f32x4 m0 = *(LAS f32x4*)(sm + L_ML), m1 = *(LAS f32x4*)(sm + L_ML + 4);
    Mwg = fmaxf(fmaxf(fmaxf(m0[0], m0[1]), fmaxf(m0[2], m0[3])), fmaxf(fmaxf(m1[0], m1[1]), fmaxf(m1[2], m1[3])));
    float l = 0.f;
#pragma unroll
    for (int k = 0; k < 4; ++k) {
      s0[k] = __builtin_amdgcn_exp2f((s0[k] - Mwg) * L2E); s1[k] = __builtin_amdgcn_exp2f((s1[k] - Mwg) * L2E);
      l += s0[k] + s1[k];
    }
    *(LAS f32x4*)(sc + seg * 8) = s0; *(LAS f32x4*)(sc + seg * 8 + 4) = s1;
    l = wave_sum(l);
    if (lane == 0) sm[L_ML + 8 + wave] = l;
    lds_barrier();
    const f32x4 l0 = *(LAS f32x4*)(sm + L_ML + 8), l1 = *(LAS f32x4*)(sm + L_ML + 12);
    const float Lwg = ((l0[0] + l0[1]) + (l0[2] + l0[3])) + ((l1[0] + l1[1]) + (l1[2] + l1[3]));
    __builtin_amdgcn_raw_buffer_store_b64(u32x2{seal(Mwg), seal(Lwg)}, rw, tid == 0 ? 0 : OOB, (WS_A + b * 256 + DH) * 4, SC1);
    SB();
  }
  f32x2 acc[12];
#pragma unroll
  for (int k = 0; k < 12; ++k) acc[k] = f32x2{0.f, 0.f};
#define V_ITER(SITE, it_, REISSUE, EXTRA)                                                              \
  do {                                                                                             \
    float pv[RD];                                                                                  \
    _Pragma("unroll") for (int j = 0; j < RD; j += 4) {                                            \
      const f32x4 t = *(LAS f32x4*)(sc + (it_) * RD + j);                                          \
      pv[j] = t[0]; pv[j + 1] = t[1]; pv[j + 2] = t[2]; pv[j + 3] = t[3];                          \
    }                                                                                              \
    _Pragma("unroll") for (int j = 0; j < RD; ++j)                                                 \
      _Pragma("unroll") for (int i = 0; i < 3; ++i) {                                              \
        axpy8<SITE>(acc + i * 4, pv[j], ring[j][i]);                                                     \
        if (REISSUE) ring[j][i] = __builtin_amdgcn_raw_buffer_load_b128(rv, voff + i * 128, SLOT_OFF((it_) + 1, j), V_AUX); \
        { const int step = j * 3 + i; (void)step; EXTRA; }                                         \
        SB();                                                                                      \
      }                                                                                            \
  } while (0)
  for (int it = 0; it < NIT - 3; ++it) {
    V_ITER(1, it, true, );
    if (LOOP_BAR) __builtin_amdgcn_s_barrier();
  }
  float cfac;
  {
    const unsigned vml = (((lane & 7) * 32) * 256 + ((lane >> 3) & 1)) * 4;
    unsigned mlv = __builtin_amdgcn_raw_buffer_load_b32(rw, vml, (WS_A + h * 256 + DH) * 4, SC1);
    unsigned tk = 0;
    if (wave == 0) asm volatile("s_mov_b64 exec, 1\n\tglobal_atomic_add %0, %1, %2, %3 sc0\n\ts_mov_b64 exec, -1" : "=&v"(tk) : "v"(0u), "v"(1u), "s"(p.ws + WS_T) : "memory");
    SB();
    V_ITER(2, NIT - 3, true, );
    asm volatile("s_waitcnt vmcnt(%1)" : "+v"(tk) : "n"(3 * RD));
    if (tid == 0) su[L_ML + 16] = tk;
    if (!none_empty(mlv)) {
      SPIN {
        REPOLL();
        mlv = __builtin_amdgcn_raw_buffer_load_b32(rw, vml, (WS_A + h * 256 + DH) * 4, SC1);
        if (none_empty(mlv)) break;
      }
    }
    const float ml = u2f(mlv);
    float M = ml;
    M = fmaxf(M, dpp<XOR1>(M)); M = fmaxf(M, dpp<XOR2>(M)); M = fmaxf(M, dpp<XOR7>(M));
    float L = __builtin_amdgcn_exp2f((ml - M) * L2E) * dpp<ROR8>(ml);
    L += dpp<XOR1>(L); L += dpp<XOR2>(L); L += dpp<XOR7>(L);
    cfac = __builtin_amdgcn_exp2f((Mwg - lane_val(M, 0)) * L2E) / lane_val(L, 0);
    PIN(cfac);
    lds_barrier();
  }
  const int mj = __builtin_amdgcn_readfirstlane(su[L_ML + 16]);
  const bool is_mlp = mj < NMLP;
  const bool up = lane & 8;
  const int k0 = (up ? 3 : 0) + ((lane & 16) ? 6 : 0) + ((lane & 32) ? 12 : 0);
  LAS float* red0 = sm + L_RED + wave * DH + seg * 8 + (k0 >> 3) * 64 + (k0 & 7);
  LAS float* red1 = sm + L_RED + wave * DH + seg * 8 + ((k0 + 1) >> 3) * 64 + ((k0 + 1) & 7);
  LAS float* red2 = sm + L_RED + wave * DH + seg * 8 + ((k0 + 2) >> 3) * 64 + ((k0 + 2) & 7);
  PIN(red0); PIN(red1); PIN(red2);
#define WAVE_PARTIAL()                                                                             \
  do {                                                                                             \
    float a_[12], c_[6];                                                                           \
    _Pragma("unroll") for (int k = 0; k < 12; ++k) a_[k] = tr32(acc[k >> 1][k & 1], acc[(k + 12) >> 1][k & 1]); \
    _Pragma("unroll") for (int k = 0; k < 6; ++k) c_[k] = tr16(a_[k], a_[k + 6]);                  \
    *red0 = (up ? c_[3] : c_[0]) + dpp<ROR8>(up ? c_[0] : c_[3]);                                  \
    *red1 = (up ? c_[4] : c_[1]) + dpp<ROR8>(up ? c_[1] : c_[4]);                                  \
    *red2 = (up ? c_[5] : c_[2]) + dpp<ROR8>(up ? c_[2] : c_[5]);                                  \
    lds_barrier();                                                                                 \
  } while (0)

  if (wave < 3) {
    V_ITER(3, NIT - 2, true, );
    __builtin_amdgcn_s_barrier();
    V_ITER(4, NIT - 1, false, );
    STAMP0(3);
    WAVE_PARTIAL();
    float t[NW];
#pragma unroll
    for (int w = 0; w < NW; ++w) t[w] = sm[L_RED + w * DH + tid];
    const float part = (((t[0] + t[1]) + (t[2] + t[3])) + ((t[4] + t[5]) + (t[6] + t[7]))) * cfac;
    __builtin_amdgcn_raw_buffer_store_b32(seal(part), rw, tid * 4, (WS_A + b * 256) * 4, SC1);
    STAMP0(4);
    unsigned v[NSPLIT];
    SPIN {
#pragma unroll
      for (int j = 0; j < NSPLIT; ++j) v[j] = __builtin_amdgcn_raw_buffer_load_b32(rw, tid * 4, (WS_A + (h + 32 * j) * 256) * 4, SC1);
      if (none_empty(umax(umax(umax(v[0], v[1]), umax(v[2], v[3])), umax(umax(v[4], v[5]), umax(v[6], v[7]))))) break;
      REPOLL();
    }
    const float o = ((u2f(v[0]) + u2f(v[1])) + (u2f(v[2]) + u2f(v[3]))) + ((u2f(v[4]) + u2f(v[5])) + (u2f(v[6]) + u2f(v[7])));
    ((LAS __bf16*)(su + L_O))[tid] = (__bf16)o;
    lds_barrier();
    if (is_mlp) { __builtin_amdgcn_s_barrier(); __builtin_amdgcn_s_barrier(); }
    __builtin_amdgcn_endpgm();
  }

  const int g = wave - 3;
  LAS unsigned* stage = su + L_W + g * 2048;
  const auto ro = __builtin_amdgcn_make_buffer_rsrc((void*)p.wo, 0, HD * D * 2, 0x00020000);
#define LAST_AND_WO(SITE)                                                                          \
  do {                                                                                             \
    const unsigned vo = (unsigned)((lane >> 1) * (D * 2) + (lane & 1) * 16);                       \
    u32x4 wor[6];                                                                                  \
    if (WO_FIRST) {                                                                                \
      _Pragma("unroll") for (int i = 0; i < 6; ++i) { wor[i] = __builtin_amdgcn_raw_buffer_load_b128(ro, vo, (h * DH + i * 32) * (D * 2) + s * 160 + g * 32, W_AUX); SB(); } \
    }                                                                                              \
    V_ITER(SITE, NIT - 1, false,                                                                   \
      if (!WO_FIRST && step < 6) wor[step] = __builtin_amdgcn_raw_buffer_load_b128(ro, vo, (h * DH + step * 32) * (D * 2) + s * 160 + g * 32, W_AUX)); \
    WAVE_PARTIAL();                                                                                \
    bf16x8 wof[6], of[6];                                                                          \
    _Pragma("unroll") for (int i = 0; i < 6; ++i) *(LAS u32x4*)(stage + (i * 64 + lane) * 4) = wor[i]; \
    _Pragma("unroll") for (int t = 0; t < 6; ++t) wof[t] = frag_tr(stage, lane, t);                \
    unsigned vyb = (lane & 15) == 0 ? lane : OOB;                                                  \
    PIN(vyb);                                                                                      \
    lds_barrier();                                                                                 \
    STAMP3(5);                                                                                     \
    _Pragma("unroll") for (int t = 0; t < 6; ++t) of[t] = frag_vec(su + L_O, lane, t);             \
    SB();                                                                                          \
    f32x4 y0 = {0.f, 0.f, 0.f, 0.f}, y1 = {0.f, 0.f, 0.f, 0.f};                                    \
    _Pragma("unroll") for (int t = 0; t < 6; t += 2) {     \
      y0 = __builtin_amdgcn_mfma_f32_16x16x32_bf16(wof[t], of[t], y0, 0, 0, 0);                    \
      y1 = __builtin_amdgcn_mfma_f32_16x16x32_bf16(wof[t + 1], of[t + 1], y1, 0, 0, 0);            \
    }                                                                                              \
    y0 += y1;                                                                                      \
    __builtin_amdgcn_raw_buffer_store_b128(u32x4{seal(y0[0]), seal(y0[1]), seal(y0[2]), seal(y0[3])}, rw, vyb, (WS_B + h * D + s * 80 + g * 16) * 4, SC1); \
    STAMP3(6);                                                                                     \
  } while (0)

  if (!is_mlp) {
    V_ITER(5, NIT - 2, true, );
    __builtin_amdgcn_s_barrier();
    LAST_AND_WO(6);
    __builtin_amdgcn_endpgm();
  }

  u32x4 wgr[4], wur[4];
  unsigned wdr[16];
  const unsigned xp = ((const unsigned*)p.x)[g * 64 + 2 * (lane & 31) + (lane >> 5)];
  {
    const auto rg_ = __builtin_amdgcn_make_buffer_rsrc((void*)p.wg, 0, D * F * 2, 0x00020000);
    const auto ru_ = __builtin_amdgcn_make_buffer_rsrc((void*)p.wu, 0, D * F * 2, 0x00020000);
    const auto rd_ = __builtin_amdgcn_make_buffer_rsrc((void*)p.wd, 0, D * F * 2, 0x00020000);
    const unsigned vg = (unsigned)((lane >> 1) * (F * 2) + (lane & 1) * 16);
    V_ITER(7, NIT - 2, true,
      if (step < 4) {
        wgr[step] = __builtin_amdgcn_raw_buffer_load_b128(rg_, vg, (g * 128 + step * 32) * (F * 2) + mj * (FPW * 2), W_AUX);
        wur[step] = __builtin_amdgcn_raw_buffer_load_b128(ru_, vg, (g * 128 + step * 32) * (F * 2) + mj * (FPW * 2), W_AUX);
      } else if (step < 12) {
        wdr[2 * step - 8] = __builtin_amdgcn_raw_buffer_load_b32(rd_, lane * 4, (mj * FPW + 2 * step - 8) * (D * 2) + g * 256, W_AUX);
        wdr[2 * step - 7] = __builtin_amdgcn_raw_buffer_load_b32(rd_, lane * 4, (mj * FPW + 2 * step - 7) * (D * 2) + g * 256, W_AUX);
      });
  }
  __builtin_amdgcn_s_barrier();
  LAST_AND_WO(8);
  bf16x8 wgf[4], wuf[4];
  unsigned wd0[8], wd1[8], hp;
#pragma unroll
  for (int i = 0; i < 4; ++i) { *(LAS u32x4*)(stage + (i * 64 + lane) * 4) = wgr[i]; *(LAS u32x4*)(stage + 1024 + (i * 64 + lane) * 4) = wur[i]; }
#pragma unroll
  for (int t = 0; t < 4; ++t) { wgf[t] = frag_tr(stage, lane, t); wuf[t] = frag_tr(stage + 1024, lane, t); }
#pragma unroll
  for (int i = 0; i < 8; ++i) {
    wd0[i] = __builtin_amdgcn_perm(wdr[2 * i + 1], wdr[2 * i], 0x05040100u); wd1[i] = __builtin_amdgcn_perm(wdr[2 * i + 1], wdr[2 * i], 0x07060302u);
    PIN(wd0[i]); PIN(wd1[i]);
  }
  float xlo = bflo(xp), xhi = bfhi(xp);
  LAS unsigned* hslot = su + g * 64 + 2 * (lane & 31) + (lane >> 5);
  PIN(xlo); PIN(xhi); PIN(hslot);
  {
    f32x4 S[4];
    unsigned need = 15;
    const unsigned vb = (unsigned)((lane >> 5) * D + g * 128 + (lane & 31) * 4) * 4;
    SPIN {
      u32x4 v[4][4];
#pragma unroll
      for (int q = 0; q < 4; ++q)
        if (need & (1u << q)) {
#pragma unroll
          for (int j = 0; j < 4; ++j) v[q][j] = __builtin_amdgcn_raw_buffer_load_b128(rw, vb, (WS_B + (2 * q + 8 * j) * D) * 4, SC1);
        }
#pragma unroll
      for (int q = 0; q < 4; ++q)
        if (need & (1u << q)) {
          if (none_empty(umax(umax(umax4(v[q][0]), umax4(v[q][1])), umax(umax4(v[q][2]), umax4(v[q][3]))))) {
            S[q] = (__builtin_bit_cast(f32x4, v[q][0]) + __builtin_bit_cast(f32x4, v[q][1])) + (__builtin_bit_cast(f32x4, v[q][2]) + __builtin_bit_cast(f32x4, v[q][3]));
            need &= ~(1u << q);
          }
        }
      if (!need) break;
      REPOLL();
    }
    STAMP3(7);
    const f32x4 y = (S[0] + S[1]) + (S[2] + S[3]);
    const float ya = tr32(y[0], y[2]), yb = tr32(y[1], y[3]);
    const unsigned yp = pack_bf(ya, yb);
    hp = pack_bf(xlo + bflo(yp), xhi + bfhi(yp));
    hslot[L_H] = hp;
  }
  lds_barrier();
  {
    unsigned hr[5];
#pragma unroll
    for (int j = 0; j < 5; ++j) hr[j] = su[L_H + j * 64 + lane];
    float m, rinv;
    ln_stats(hr, m, rinv);
    const unsigned dp = pack_bf(bflo(hp) - m, bfhi(hp) - m);
    hslot[L_XN] = pack_bf(bflo(dp) * rinv, bfhi(dp) * rinv);
    bf16x8 xf[4];
#pragma unroll
    for (int t = 0; t < 4; ++t) xf[t] = frag_vec(su + L_XN + g * 64, lane, t);
    SB();
    f32x4 ga = {0.f, 0.f, 0.f, 0.f}, ua = {0.f, 0.f, 0.f, 0.f};
#pragma unroll
    for (int t = 0; t < 4; ++t) {
      ga = __builtin_amdgcn_mfma_f32_16x16x32_bf16(xf[t], wgf[t], ga, 0, 0, 0);
      ua = __builtin_amdgcn_mfma_f32_16x16x32_bf16(xf[t], wuf[t], ua, 0, 0, 0);
    }
    sm[L_GU + g * 32 + (lane & 31)] = (lane & 16) ? ua[0] : ga[0];
  }
  lds_barrier();
  {
    const int n = lane & 15;
    float gs[5], us[5];
#pragma unroll
    for (int w = 0; w < 5; ++w) { gs[w] = sm[L_GU + w * 32 + n]; us[w] = sm[L_GU + w * 32 + 16 + n]; }
    const float gt = rb(((gs[0] + gs[1]) + (gs[2] + gs[3])) + gs[4]), ut = rb(((us[0] + us[1]) + (us[2] + us[3])) + us[4]);
    const float act = rb(__builtin_amdgcn_rcpf(1.f + __builtin_amdgcn_exp2f(-L2E * gt))) * ut;
    const unsigned ap = pack_bf(act, dpp<XOR1>(act));
    unsigned a2[8];
#pragma unroll
    for (int i = 0; i < 8; ++i) a2[i] = __builtin_amdgcn_readlane(ap, 2 * i);
    SB();
    float d0 = 0.f, d1 = 0.f;
#pragma unroll
    for (int i = 0; i < 8; ++i) { d0 = dot2(wd0[i], a2[i], d0); d1 = dot2(wd1[i], a2[i], d1); }
    const unsigned e0 = seal(d0), e1 = seal(d1);
    __builtin_amdgcn_raw_buffer_store_b128(u32x4{e0, e1, f2u(dpp<XOR1>(u2f(e0))), f2u(dpp<XOR1>(u2f(e1)))}, rw, (lane & 1) ? OOB : lane * 8, (WS_D + mj * D + g * 128) * 4, SC1);
  }
  STAMP3(8);
  if (mj >= NFIN || wave != 3) return;
  {
    const int col = (lane & 3) * 4 + ((lane >> 4) & 1) + 2 * (lane >> 5);
    const unsigned hw = su[L_H + mj * 8 + (col >> 1)];
    float hv = (col & 1) ? bfhi(hw) : bflo(hw);
    unsigned short* op = p.out + mj * 16 + col;
    PIN(hv); PIN(op);
    const unsigned vd = (unsigned)((lane >> 2) * D + (lane & 3) * 4) * 4;
    u32x4 v[6];
    SPIN {
#pragma unroll
      for (int i = 0; i < 6; ++i) v[i] = __builtin_amdgcn_raw_buffer_load_b128(rw, vd, (WS_D + i * 16 * D + mj * 16) * 4, SC1);
      if (none_empty(umax(umax(umax(umax4(v[0]), umax4(v[1])), umax(umax4(v[2]), umax4(v[3]))), umax(umax4(v[4]), umax4(v[5]))))) break;
      REPOLL();
    }
    const f32x4 t = ((__builtin_bit_cast(f32x4, v[0]) + __builtin_bit_cast(f32x4, v[1])) + (__builtin_bit_cast(f32x4, v[2]) + __builtin_bit_cast(f32x4, v[3]))) +
                    (__builtin_bit_cast(f32x4, v[4]) + __builtin_bit_cast(f32x4, v[5]));
    float e = tr16(tr32(t[0], t[2]), tr32(t[1], t[3]));
    e += dpp<ROR8>(e); e += dpp<ROR4>(e);
    const __bf16 o = (__bf16)(hv + rb(e));
    if ((lane & 12) == 0) *(GAS unsigned short*)op = __builtin_bit_cast(unsigned short, o);
    if (mj == 0) __builtin_amdgcn_raw_buffer_store_b64(u32x2{tag_lo, tag_hi}, rw, lane == 0 ? 0 : OOB, WS_E * 4, SC1);
  }
  STAMP3(9);
}

}

extern "C" void kernel_launch(void* const* d_in, const int* in_sizes, int n_in, void* d_out, int out_size, void* d_ws, size_t ws_size,
                              hipStream_t stream) {
  Params p;
  p.x = (const unsigned short*)d_in[0];
  p.kc = (const unsigned short*)d_in[1];
  p.vc = (const unsigned short*)d_in[2];
  p.wq = (const unsigned short*)d_in[3];
  p.wo = (const unsigned short*)d_in[4];
  p.wg = (const unsigned short*)d_in[6];
  p.wu = (const unsigned short*)d_in[7];
  p.wd = (const unsigned short*)d_in[8];
  p.out = (unsigned short*)d_out;
  p.ws = (unsigned*)d_ws;
  (void)hipFuncSetAttribute((const void*)fused_decode, hipFuncAttributeMaxDynamicSharedMemorySize, LDS_BYTES);
  fused_decode<<<NWG, NTH, LDS_BYTES, stream>>>(p);
}
```

```cpp
#include <hip/hip_runtime.h>
#include <hip/hip_bf16.h>
#include <stdint.h>

namespace {

constexpr int D = 640, H = 32, SK = 32768, DH = 192, F = 1536, HD = H * DH;
constexpr int NWG = 256, NW = 8, NTH = NW * 64;
constexpr int RD = 4;
constexpr unsigned TILE_B = 8 * DH * 2;
constexpr unsigned CHUNK_B = NW * RD * TILE_B;
constexpr unsigned HEAD_B = SK * DH * 2;
constexpr unsigned KV_BYTES = H * HEAD_B;
constexpr int NTASK = SK / 256;
constexpr int NMLP = 96, FPW = F / NMLP;
constexpr int NFIN = 40;
constexpr int RING0 = 2;
constexpr int STOP_LATE = 76, STOP_WO = 100;

constexpr int WS_E = 0;
constexpr int WS_T = 64;
constexpr int WS_F = 1024;
constexpr int WS_ML = WS_F + 512;
constexpr int WS_Q = WS_ML + H * 64;
constexpr int WS_B = WS_Q + NWG * 2 * DH;
constexpr int WS_H = WS_B + H * D;
constexpr int WS_D = WS_H + D;
constexpr int WS_P = WS_D + NMLP * D;
constexpr int WS_S = WS_P + 4 * SK;
constexpr int WS_END = WS_S + H * NTASK * DH;
constexpr unsigned OOB = 0x80000000u;
constexpr unsigned SPIN_CAP = 1u << 20;
static_assert(WS_Q % 4 == 0 && WS_B % 4 == 0 && WS_H % 4 == 0 && WS_D % 4 == 0 && WS_P % 4 == 0 && WS_S % 4 == 0, "16-byte pieces");

constexpr int SC_STR = 68;
constexpr int L_SC = 0;
constexpr int L_RED = L_SC + NW * 8 * SC_STR;
constexpr int L_Q = L_RED + 16 * DH;
constexpr int L_ML = L_Q + 4 * 96;
constexpr int L_TK = L_ML + 64;
constexpr int L_O = L_TK + 4;
constexpr int L_H = L_O + 96;
constexpr int L_X = L_H + 320;
constexpr int L_XN = L_X + 320;
constexpr int L_GU = L_XN + 176;
constexpr int L_WO = L_GU + 160;
constexpr int L_WG = L_WO;
constexpr int L_WD = L_WG + 2 * D * 8;
constexpr int L_END = L_WD + FPW * D / 2;
constexpr int LDS_BYTES = 100 * 1024;
static_assert(L_END * 4 <= LDS_BYTES && (L_WO + DH * 64) * 4 <= LDS_BYTES && LDS_BYTES > 81920 && LDS_BYTES <= 163840 && L_WO % 4 == 0 && L_O % 4 == 0 && L_XN % 4 == 0 && L_Q % 4 == 0 && L_WG % 4 == 0 && L_WD % 4 == 0, "lds");

typedef unsigned u32x4 __attribute__((ext_vector_type(4)));
typedef unsigned u32x2 __attribute__((ext_vector_type(2)));
typedef float f32x4 __attribute__((ext_vector_type(4)));
typedef float f32x2 __attribute__((ext_vector_type(2)));
typedef __bf16 bf16x2 __attribute__((ext_vector_type(2)));
typedef __bf16 bf16x8 __attribute__((ext_vector_type(8)));
typedef short s16x4 __attribute__((ext_vector_type(4)));
#define LAS __attribute__((address_space(3)))
#define GAS __attribute__((address_space(1)))
#define CAS __attribute__((address_space(4)))
#define RLX_AGENT __ATOMIC_RELAXED, __HIP_MEMORY_SCOPE_AGENT
#define SB() __builtin_amdgcn_sched_barrier(0)
#define PIN(x) asm volatile("" : "+v"(x))
#define REPOLL() asm volatile("s_sleep 1" ::: "memory")
#define SPIN _Pragma("nounroll") for (unsigned spins = 0; spins < SPIN_CAP; ++spins)
#define UNROLL _Pragma("unroll")
#define LD128(rs, vo, so, aux) __builtin_amdgcn_raw_buffer_load_b128(rs, vo, so, aux)
constexpr int SC1 = 16, NT = 2;
constexpr float L2E = 1.4426950408889634f;

__device__ __forceinline__ float u2f(unsigned u) { return __builtin_bit_cast(float, u); }
__device__ __forceinline__ unsigned f2u(float f) { return __builtin_bit_cast(unsigned, f); }
__device__ __forceinline__ f32x4 as_f4(u32x4 v) { return __builtin_bit_cast(f32x4, v); }
__device__ __forceinline__ float bflo(unsigned u) { return u2f(u << 16); }
__device__ __forceinline__ float bfhi(unsigned u) { return u2f(u & 0xffff0000u); }
__device__ __forceinline__ float rb(float x) { return (float)(__bf16)x; }
__device__ __forceinline__ unsigned pack_bf(float lo, float hi) {
  bf16x2 v = {(__bf16)lo, (__bf16)hi};
  return __builtin_bit_cast(unsigned, v);
}
__device__ __forceinline__ float dot2(unsigned k, unsigned q, float acc) {
  return __builtin_amdgcn_fdot2_f32_bf16(__builtin_bit_cast(bf16x2, k), __builtin_bit_cast(bf16x2, q), acc, false);
}
template <int CTRL> __device__ __forceinline__ float dpp(float x) {
  return u2f(__builtin_amdgcn_mov_dpp(f2u(x), CTRL, 0xf, 0xf, true));
}
constexpr int XOR1 = 0xB1, XOR2 = 0x4E, XOR7 = 0x141, ROR8 = 0x128, ROR4 = 0x124;
__device__ __forceinline__ float lane_val(float v, int l) { return u2f(__builtin_amdgcn_readlane(f2u(v), l)); }
__device__ __forceinline__ float tr32(float a, float b) {
  auto r = __builtin_amdgcn_permlane32_swap(f2u(a), f2u(b), false, false);
  return u2f(r[0]) + u2f(r[1]);
}
__device__ __forceinline__ float tr16(float a, float b) {
  auto r = __builtin_amdgcn_permlane16_swap(f2u(a), f2u(b), false, false);
  return u2f(r[0]) + u2f(r[1]);
}
__device__ __forceinline__ float wave_sum(float v) {
  v += dpp<XOR1>(v); v += dpp<XOR2>(v); v += dpp<XOR7>(v); v += dpp<ROR8>(v);
  v += u2f(__builtin_amdgcn_update_dpp(0, f2u(v), 0x142, 0xa, 0xf, false));
  v += u2f(__builtin_amdgcn_update_dpp(0, f2u(v), 0x143, 0xc, 0xf, false));
  return lane_val(v, 63);
}
__device__ __forceinline__ float grp_max(float v) {
  v = fmaxf(v, dpp<XOR1>(v)); v = fmaxf(v, dpp<ROR8>(v));
  auto r = __builtin_amdgcn_permlane16_swap(f2u(v), f2u(v), false, false);
  v = fmaxf(u2f(r[0]), u2f(r[1]));
  auto t = __builtin_amdgcn_permlane32_swap(f2u(v), f2u(v), false, false);
  return fmaxf(u2f(t[0]), u2f(t[1]));
}
__device__ __forceinline__ float grp_sum(float v) {
  v += dpp<XOR1>(v); v += dpp<ROR8>(v);
  v = tr16(v, v);
  return tr32(v, v);
}
__device__ __forceinline__ void lds_barrier() {
  asm volatile("s_waitcnt lgkmcnt(0)" ::: "memory");
  __builtin_amdgcn_s_barrier();
  asm volatile("" ::: "memory");
}
__device__ __forceinline__ unsigned mark(float v, unsigned t) { return (f2u(v) & 0xFFFFFF00u) | t; }
__device__ __forceinline__ unsigned or4(u32x4 v) { return (v[0] | v[1]) | (v[2] | v[3]); }
__device__ __forceinline__ bool fresh(unsigned bits) { return __builtin_amdgcn_ballot_w64((bits & 0xFFu) != 0) == 0; }
__device__ __forceinline__ bool none(bool c) { return __builtin_amdgcn_ballot_w64(c) == 0; }

__device__ __forceinline__ void axpy8(f32x2* acc, float pv, u32x4 w) {
  asm volatile("" : "+v"(w));
  const f32x2 pp = {pv, pv};
#pragma unroll
  for (int k = 0; k < 4; ++k) acc[k] = __builtin_elementwise_fma(pp, f32x2{bflo(w[k]), bfhi(w[k])}, acc[k]);
  asm volatile("" : "+v"(acc[0]), "+v"(acc[1]), "+v"(acc[2]), "+v"(acc[3]));
}

__device__ __forceinline__ void ln_stats(const unsigned (&pr)[5], float& m, float& rinv) {
  float v[10];
#pragma unroll
  for (int j = 0; j < 5; ++j) { v[2 * j] = bflo(pr[j]); v[2 * j + 1] = bfhi(pr[j]); }
  const float s = ((v[0] + v[1]) + (v[2] + v[3])) + ((v[4] + v[5]) + (v[6] + v[7])) + (v[8] + v[9]);
  const float mean = wave_sum(s) * (1.f / D);
  float q0 = 0.f, q1 = 0.f;
#pragma unroll
  for (int j = 0; j < 5; ++j) { const float d0 = v[2 * j] - mean, d1 = v[2 * j + 1] - mean; q0 = fmaf(d0, d0, q0); q1 = fmaf(d1, d1, q1); }
  const float var = rb(wave_sum(q0 + q1) * (1.f / D));
  m = rb(mean);
  rinv = __builtin_amdgcn_rcpf(rb(__builtin_amdgcn_sqrtf(rb(var + 1e-5f))));
}

__device__ __forceinline__ f32x4 wsum4(const u32x4 (&v)[4], const float (&w)[4]) {
  const f32x4 w0 = {w[0], w[0], w[0], w[0]}, w2 = {w[2], w[2], w[2], w[2]};
  return __builtin_elementwise_fma(as_f4(v[0]), w0, as_f4(v[1]) * w[1]) + __builtin_elementwise_fma(as_f4(v[2]), w2, as_f4(v[3]) * w[3]);
}

template <int ROWB> __device__ __forceinline__ bf16x8 frag_tr(LAS unsigned char* img, int lane) {
  LAS unsigned char* a = img + (8 * (lane >> 4) + ((lane >> 2) & 3)) * ROWB + (lane & 3) * 8;
  const s16x4 lo = __builtin_amdgcn_ds_read_tr16_b64_v4i16((LAS s16x4*)a);
  const s16x4 hi = __builtin_amdgcn_ds_read_tr16_b64_v4i16((LAS s16x4*)(a + 4 * ROWB));
  return __builtin_bit_cast(bf16x8, __builtin_shufflevector(lo, hi, 0, 1, 2, 3, 4, 5, 6, 7));
}
__device__ __forceinline__ bf16x8 frag_vec(LAS unsigned* vec, int lane, int t) {
  return __builtin_bit_cast(bf16x8, *(LAS u32x4*)(vec + 16 * t + 4 * (lane >> 4)));
}

struct Params {
  const unsigned short *x, *kc, *vc, *wq, *wo, *wg, *wu, *wd;
  unsigned short* out;
  unsigned* ws;
};

__global__ void __launch_bounds__(NTH, 2) fused_decode(Params p) {
  extern __shared__ __attribute__((aligned(16))) float smem_[];
  LAS float* sm = (LAS float*)smem_;
  LAS unsigned* su = (LAS unsigned*)smem_;
  const int tid = threadIdx.x, lane = tid & 63, wave = __builtin_amdgcn_readfirstlane(tid >> 6);
  const int b = blockIdx.x, x = b & 7, m = b >> 3;
  const int r = lane >> 3, seg = lane & 7;
  const int pool = b >> 5, rho = (m + 4 * x) & 31;
  const bool is_mlp = rho >= 20, late = rho < 5;
  const int hd = x + (late ? 24 : 8 * ((rho - 5) / 5)), sl = rho % 5;
  const int mj = x * 12 + rho - 20;
  const int stop_at = late ? STOP_LATE : is_mlp ? NTASK : STOP_WO;

  const auto rk = __builtin_amdgcn_make_buffer_rsrc((void*)p.kc, 0, (int)KV_BYTES, 0x00020000);
  const auto rv = __builtin_amdgcn_make_buffer_rsrc((void*)p.vc, 0, (int)KV_BYTES, 0x00020000);
  const auto rw = __builtin_amdgcn_make_buffer_rsrc((void*)p.ws, 0, WS_END * 4, 0x00020000);
  const unsigned voff = r * (DH * 2) + seg * 16;
  const unsigned so0 = x * HEAD_B + m * CHUNK_B + wave * (RD * TILE_B);
#define IT_OFF(it_) (so0 + ((it_) >> 2) * (8 * HEAD_B) + ((it_) & 3) * (32 * CHUNK_B))

  unsigned xr[5];
  UNROLL for (int j = 0; j < 5; ++j) xr[j] = ((const unsigned*)p.x)[j * 64 + lane];
  SB();
  const int qrr = lane >= 24, qsg = lane - 24 * qrr;
  u32x4 wqr[5];
  {
    const auto rq = __builtin_amdgcn_make_buffer_rsrc((void*)p.wq, 0, D * HD * 2, 0x00020000);
    UNROLL for (int j = 0; j < 5; ++j) {
      wqr[j] = LD128(rq, lane < 48 ? qrr * (HD * 2) + qsg * 16 : OOB, ((b >> 5) * 80 + wave * 10 + 2 * j) * (HD * 2) + (b & 31) * (DH * 2), 0);
      SB();
    }
  }
  const unsigned long long ep = __hip_atomic_load((GAS unsigned long long*)(p.ws + WS_E), RLX_AGENT);
  SB();
  __builtin_amdgcn_s_barrier();
  u32x4 ring[RD][3];
  UNROLL for (int j = 0; j < RING0; ++j)
    UNROLL for (int i = 0; i < 3; ++i) { ring[j][i] = LD128(rk, voff + i * 128, so0 + j * TILE_B, NT); SB(); }
  {
    const CAS unsigned* xs = (const CAS unsigned*)p.x + (b >> 5) * 40 + wave * 5;
    float mu, rinv;
    ln_stats(xr, mu, rinv);
    if (wave == 7) { UNROLL for (int j = 0; j < 5; ++j) su[L_X + j * 64 + lane] = xr[j]; }
    f32x2 a4[4];
    UNROLL for (int k = 0; k < 4; ++k) a4[k] = f32x2{0.f, 0.f};
    UNROLL for (int j = 0; j < 5; ++j) {
      const unsigned xp = xs[j];
      const float xv = qrr ? bfhi(xp) : bflo(xp);
      axpy8(a4, rb(rb(xv - mu) * rinv), wqr[j]);
    }
    if (lane < 48) {
      LAS float* dst = sm + L_RED + (wave * 2 + qrr) * DH + qsg * 8;
      *(LAS f32x4*)dst = f32x4{a4[0][0], a4[0][1], a4[1][0], a4[1][1]};
      *(LAS f32x4*)(dst + 4) = f32x4{a4[2][0], a4[2][1], a4[3][0], a4[3][1]};
    }
  }
  const unsigned tag_lo = __builtin_amdgcn_readfirstlane((unsigned)(ep + 1)), tag_hi = __builtin_amdgcn_readfirstlane((unsigned)((ep + 1) >> 32));
  const u32x4 tag4 = {tag_lo & 0xFFu, ((tag_lo >> 8) + tag_lo) & 0xFFu, ((tag_lo >> 16) + tag_lo) & 0xFFu, ((tag_lo >> 24) + tag_lo) & 0xFFu};
  lds_barrier();
  if (wave < 3) {
    float t0 = 0.f, t1 = 0.f;
    UNROLL for (int w = 0; w < 16; w += 2) { t0 += sm[L_RED + w * DH + tid]; t1 += sm[L_RED + (w + 1) * DH + tid]; }
    __builtin_amdgcn_raw_buffer_store_b64(u32x2{f2u(t0 + t1), tag_lo}, rw, tid * 8, WS_Q * 4 + b * (DH * 8), SC1);
  }
#define Q_LOAD(vq_) UNROLL for (int j = 0; j < 8; ++j) { g[j] = LD128(rw, vq_, WS_Q * 4 + (x + 32 * j) * (DH * 8), SC1); SB(); }
#define Q_BAD() (!none((onm & ((g[0][1] ^ tag_lo) | (g[0][3] ^ tag_lo) | (g[1][1] ^ tag_lo) | (g[1][3] ^ tag_lo) | (g[2][1] ^ tag_lo) | (g[2][3] ^ tag_lo) | \
                               (g[3][1] ^ tag_lo) | (g[3][3] ^ tag_lo) | (g[4][1] ^ tag_lo) | (g[4][3] ^ tag_lo) | (g[5][1] ^ tag_lo) | (g[5][3] ^ tag_lo) | \
                               (g[6][1] ^ tag_lo) | (g[6][3] ^ tag_lo) | (g[7][1] ^ tag_lo) | (g[7][3] ^ tag_lo))) != 0))
#define Q_FINISH(vq_, slot_)                                                                       \
  do {                                                                                             \
    unsigned onm = on ? ~0u : 0u;                                                                  \
    PIN(onm);                                                                                      \
    if (Q_BAD()) { SPIN { REPOLL(); Q_LOAD(vq_); if (!Q_BAD()) break; } }                          \
    const float q0 = ((u2f(g[0][0]) + u2f(g[1][0])) + (u2f(g[2][0]) + u2f(g[3][0]))) + ((u2f(g[4][0]) + u2f(g[5][0])) + (u2f(g[6][0]) + u2f(g[7][0]))); \
    const float q1 = ((u2f(g[0][2]) + u2f(g[1][2])) + (u2f(g[2][2]) + u2f(g[3][2]))) + ((u2f(g[4][2]) + u2f(g[5][2])) + (u2f(g[6][2]) + u2f(g[7][2]))); \
    unsigned qq = pack_bf(q0, q1);                                                                 \
    PIN(qq);                                                                                       \
    if (on) su[slot_] = qq;                                                                        \
  } while (0)
  __builtin_amdgcn_s_sleep(16);
  {
    u32x4 g[8];
    const bool on = tid < 96;
    const unsigned vq = on ? (unsigned)tid * 16 : OOB;
    if (wave < 2) { Q_LOAD(vq); }
    __builtin_amdgcn_s_barrier();
    UNROLL for (int j = RING0; j < RD; ++j)
      UNROLL for (int i = 0; i < 3; ++i) { ring[j][i] = LD128(rk, voff + i * 128, so0 + j * TILE_B, NT); SB(); }
    if (wave < 2) Q_FINISH(vq, L_Q + tid);
  }
  lds_barrier();
  u32x4 qp[3];
#define Q_FRAG(j_) UNROLL for (int i = 0; i < 3; ++i) qp[i] = *(LAS u32x4*)(su + L_Q + (j_) * 96 + i * 32 + seg * 4)
  Q_FRAG(0);

  LAS float* sc = sm + L_SC + (wave * 8 + r) * SC_STR;
  const float scale = 0.07216878364870322f;
#define K_ITER(it_, RS, nso_)                                                                      \
  do {                                                                                             \
    float mine = 0.f;                                                                              \
    const unsigned ns_ = (nso_);                                                                   \
    UNROLL for (int j = 0; j < RD; ++j) {                                                          \
      float d0 = 0.f, d1 = 0.f;                                                                    \
      UNROLL for (int i = 0; i < 3; ++i) {                                                         \
        d0 = dot2(ring[j][i][0], qp[i][0], d0); d1 = dot2(ring[j][i][1], qp[i][1], d1);            \
        d0 = dot2(ring[j][i][2], qp[i][2], d0); d1 = dot2(ring[j][i][3], qp[i][3], d1);            \
        ring[j][i] = LD128(RS, voff + i * 128, ns_ + j * TILE_B, NT);                              \
        SB();                                                                                      \
      }                                                                                            \
      float d = d0 + d1;                                                                           \
      d += dpp<XOR1>(d); d += dpp<XOR2>(d); d += dpp<XOR7>(d);                                     \
      mine = ((seg & 3) == j) ? d : mine;                                                          \
    }                                                                                              \
    sc[(it_) * RD + (seg & 3)] = rb(rb(mine) * scale);                                             \
  } while (0)
  K_ITER(0, rk, IT_OFF(1));
  __builtin_amdgcn_s_barrier();
  {
    __builtin_amdgcn_raw_buffer_store_b32(0u, rw, b == 0 && tid < 8 ? (unsigned)tid * 256 : OOB, WS_T * 4, SC1);
    SB();
    u32x4 g[8];
    const bool on = tid < 288;
    const int jq = tid / 96;
    const unsigned vq = on ? (unsigned)((jq + 1) * 8 * (DH * 8) + (tid - 96 * jq) * 16) : OOB;
    Q_LOAD(vq);
    K_ITER(1, rk, IT_OFF(2));
    Q_FINISH(vq, L_Q + 96 + tid);
    lds_barrier();
  }
  _Pragma("nounroll") for (int it = 2; it < 15; ++it) {
    if ((it & 3) == 0) { Q_FRAG(it >> 2); }
    K_ITER(it, rk, IT_OFF(it + 1));
    __builtin_amdgcn_s_barrier();
  }
  K_ITER(15, rv, so0);
  {
    const int jj = seg >> 1;
    f32x4 s0 = *(LAS f32x4*)(sc + seg * 8), s1 = *(LAS f32x4*)(sc + seg * 8 + 4);
    const float mx = grp_max(fmaxf(fmaxf(fmaxf(s0[0], s0[1]), fmaxf(s0[2], s0[3])), fmaxf(fmaxf(s1[0], s1[1]), fmaxf(s1[2], s1[3]))));
    if (lane < 8) sm[L_ML + jj * 8 + wave] = mx;
    lds_barrier();
    {
      const int c = 2 * (wave & 1) + (lane >> 5);
      LAS float* src = sm + L_SC + 2 * (lane & 31) * SC_STR + 48 + 4 * c;
      const u32x4 ra = *(LAS u32x4*)src, rb_ = *(LAS u32x4*)(src + SC_STR);
      const u32x4 pk = {__builtin_amdgcn_perm(ra[1], ra[0], 0x07060302u), __builtin_amdgcn_perm(ra[3], ra[2], 0x07060302u),
                        __builtin_amdgcn_perm(rb_[1], rb_[0], 0x07060302u), __builtin_amdgcn_perm(rb_[3], rb_[2], 0x07060302u)};
      __builtin_amdgcn_raw_buffer_store_b128(pk, rw, wave < 2 ? (unsigned)((x * NTASK + m + 32 * c) * 128 + (lane & 31) * 4) * 4 : OOB, WS_P * 4, SC1);
    }
    const f32x4 m0 = *(LAS f32x4*)(sm + L_ML + jj * 8), m1 = *(LAS f32x4*)(sm + L_ML + jj * 8 + 4);
    const float Mwg = fmaxf(fmaxf(fmaxf(m0[0], m0[1]), fmaxf(m0[2], m0[3])), fmaxf(fmaxf(m1[0], m1[1]), fmaxf(m1[2], m1[3])));
    float l = 0.f;
    UNROLL for (int k = 0; k < 4; ++k) {
      s0[k] = __builtin_amdgcn_exp2f((s0[k] - Mwg) * L2E); s1[k] = __builtin_amdgcn_exp2f((s1[k] - Mwg) * L2E);
      l += s0[k] + s1[k];
    }
    if (seg < 6) { *(LAS f32x4*)(sc + seg * 8) = s0; *(LAS f32x4*)(sc + seg * 8 + 4) = s1; }
    l = grp_sum(l);
    if (lane < 8) sm[L_ML + 32 + jj * 8 + wave] = l;
    lds_barrier();
    const f32x4 l0 = *(LAS f32x4*)(sm + L_ML + 32 + jj * 8), l1 = *(LAS f32x4*)(sm + L_ML + 32 + jj * 8 + 4);
    const float Lwg = ((l0[0] + l0[1]) + (l0[2] + l0[3])) + ((l1[0] + l1[1]) + (l1[2] + l1[3]));
    __builtin_amdgcn_raw_buffer_store_b64(u32x2{f2u(Mwg), f2u(Lwg)}, rw, tid < 8 && !(tid & 1) ? (WS_ML + ((x + 8 * jj) * 32 + m) * 2) * 4 : OOB, 0, SC1);
    SB();
  }
  f32x2 acc[12];
#define ACC_ZERO() UNROLL for (int k = 0; k < 12; ++k) acc[k] = f32x2{0.f, 0.f}
  ACC_ZERO();
#define V_ITER(pv, REISSUE, nvo_, nso_, EXTRA)                                                       \
  do {                                                                                             \
    const unsigned nv_ = (nvo_), ns_ = (nso_);                                                     \
    UNROLL for (int j = 0; j < RD; ++j)                                                            \
      UNROLL for (int i = 0; i < 3; ++i) {                                                         \
        axpy8(acc + i * 4, pv[j], ring[j][i]);                                                     \
        if (REISSUE) ring[j][i] = LD128(rv, nv_ + i * 128, ns_ + j * TILE_B, NT);                  \
        { const int step = j * 3 + i; (void)step; EXTRA; }                                         \
        SB();                                                                                      \
      }                                                                                            \
  } while (0)
#define V_STATIC(it_) do { const f32x4 pv_ = *(LAS f32x4*)(sc + (it_) * RD); V_ITER(pv_, true, voff, IT_OFF((it_) + 1), ); } while (0)
  const bool up = lane & 8;
  const int k0 = (up ? 3 : 0) + ((lane & 16) ? 6 : 0) + ((lane & 32) ? 12 : 0);
  LAS float* red0 = sm + L_RED + wave * DH + seg * 8 + (k0 >> 3) * 64 + (k0 & 7);
  LAS float* red1 = sm + L_RED + wave * DH + seg * 8 + ((k0 + 1) >> 3) * 64 + ((k0 + 1) & 7);
  LAS float* red2 = sm + L_RED + wave * DH + seg * 8 + ((k0 + 2) >> 3) * 64 + ((k0 + 2) & 7);
  LAS float* redr = sm + L_RED + (tid < DH ? tid : 0);
  const unsigned vpub = tid < DH && !(tid & 3) ? (unsigned)tid * 4 : OOB;
  const unsigned tagl = (lane & 2) ? ((lane & 1) ? tag4[3] : tag4[2]) : ((lane & 1) ? tag4[1] : tag4[0]);
#define REDUCE(par_, dst_)                                                                        \
  do {                                                                                             \
    float a_[12], c_[6];                                                                           \
    const int o_ = (par_) * (NW * DH);                                                             \
    UNROLL for (int k = 0; k < 12; ++k) a_[k] = tr32(acc[k >> 1][k & 1], acc[(k + 12) >> 1][k & 1]); \
    UNROLL for (int k = 0; k < 6; ++k) c_[k] = tr16(a_[k], a_[k + 6]);                             \
    red0[o_] = (up ? c_[3] : c_[0]) + dpp<ROR8>(up ? c_[0] : c_[3]);                               \
    red1[o_] = (up ? c_[4] : c_[1]) + dpp<ROR8>(up ? c_[1] : c_[4]);                               \
    red2[o_] = (up ? c_[5] : c_[2]) + dpp<ROR8>(up ? c_[2] : c_[5]);                               \
    lds_barrier();                                                                                 \
    float t_[NW];                                                                                  \
    UNROLL for (int w = 0; w < NW; ++w) t_[w] = redr[o_ + w * DH];                                 \
    dst_ = mark(((t_[0] + t_[1]) + (t_[2] + t_[3])) + ((t_[4] + t_[5]) + (t_[6] + t_[7])), tagl);   \
  } while (0)
#define PUT(val_, vp_, slot_)     \
  do {                                                                                             \
    const u32x4 q_ = {val_, f2u(dpp<0x55>(u2f(val_))), f2u(dpp<0xAA>(u2f(val_))), f2u(dpp<0xFF>(u2f(val_)))}; \
    __builtin_amdgcn_raw_buffer_store_b128(q_, rw, vp_, (WS_S + (slot_) * DH) * 4, SC1);           \
    SB();                                                                                          \
  } while (0)
#define PUBLISH(par_, vp_, slot_) do { unsigned part_; REDUCE(par_, part_); PUT(part_, vp_, slot_); } while (0)
#define SLOT(head_, k_) ((head_) < 24 ? (((head_) & 7) * 32 + (k_)) * 3 + ((head_) >> 3) : (head_) * NTASK + (k_))

  V_STATIC(0);
  asm volatile("s_waitcnt vmcnt(12)" ::: "memory");
  __builtin_amdgcn_s_barrier();
  __builtin_amdgcn_raw_buffer_store_b64(u32x2{tag_lo, tag_hi}, rw, tid == 0 ? 0 : OOB, WS_F * 4 + b * 8, SC1);
  SB();
  _Pragma("nounroll") for (int it = 1; it < 3; ++it) { V_STATIC(it); __builtin_amdgcn_s_barrier(); }
  V_STATIC(3);
  unsigned part0, part1;
  REDUCE(0, part0);
  PIN(part0);
  ACC_ZERO();
  _Pragma("nounroll") for (int it = 4; it < 7; ++it) { V_STATIC(it); __builtin_amdgcn_s_barrier(); }
  V_STATIC(7);
  REDUCE(1, part1);
  PIN(part1);
  ACC_ZERO();
  V_STATIC(8);
  __builtin_amdgcn_s_barrier();
  unsigned tk = 0;
  unsigned* const ctr = p.ws + WS_T + pool * 64;
#define DRAW() if (wave == 0) asm volatile("s_mov_b64 exec, 1\n\tglobal_atomic_add %0, %1, %2, %3 sc0\n\ts_mov_b64 exec, -1" : "+v"(tk) : "v"(0u), "v"(1u), "s"(ctr) : "memory")
  {
    u32x4 c0 = LD128(rw, lane * 32, WS_F * 4, SC1), c1 = LD128(rw, lane * 32 + 16, WS_F * 4, SC1);
    SB();
    V_STATIC(9);
#define F_BAD() (!none((((c0[0] ^ tag_lo) | (c0[2] ^ tag_lo) | (c1[0] ^ tag_lo) | (c1[2] ^ tag_lo)) | ((c0[1] ^ tag_hi) | (c0[3] ^ tag_hi) | (c1[1] ^ tag_hi) | (c1[3] ^ tag_hi))) != 0))
    if (F_BAD()) {
      SPIN {
        REPOLL();
        c0 = LD128(rw, lane * 32, WS_F * 4, SC1); c1 = LD128(rw, lane * 32 + 16, WS_F * 4, SC1);
        if (!F_BAD()) break;
      }
    }
    __builtin_amdgcn_s_barrier();
  }
  float Mh;
  {
    u32x4 ml = LD128(rw, (unsigned)(lane & 15) * 16, (WS_ML + (24 + pool) * 64) * 4, SC1);
    DRAW();
    SB();
    V_STATIC(10);
    asm volatile("s_waitcnt vmcnt(12)" : "+v"(tk));
    if (tid == 0) su[L_TK] = tk;
    PIN(ml);
    Mh = fmaxf(u2f(ml[0]), u2f(ml[2]));
    Mh = fmaxf(Mh, dpp<XOR1>(Mh)); Mh = fmaxf(Mh, dpp<XOR2>(Mh)); Mh = fmaxf(Mh, dpp<XOR7>(Mh)); Mh = fmaxf(Mh, dpp<ROR8>(Mh));
    PIN(Mh);
    lds_barrier();
  }
  const unsigned vpp = (unsigned)(wave * 8 + r) * 8, vwv = voff + wave * (RD * TILE_B);
  f32x4 pc;
  int tn = __builtin_amdgcn_readfirstlane(su[L_TK]), tc, par = 1;
#define POOL_STEP(pv_)                                                                             \
    const bool have = tn < NTASK;                                                                  \
    const int cn = pool * NTASK + (have ? tn : 0);                                                 \
    tk = NTASK;                                                                                    \
    if (tn < stop_at) { DRAW(); }                                                                  \
    const u32x2 sn = __builtin_amdgcn_raw_buffer_load_b64(rw, have ? vpp : OOB, WS_P * 4 + cn * 512, SC1); \
    unsigned nvo = have ? vwv : OOB;                                                               \
    PIN(nvo);                                                                                      \
    SB();                                                                                          \
    V_ITER(pv_, true, nvo, 24 * HEAD_B + cn * CHUNK_B, );                                          \
    asm volatile("s_waitcnt vmcnt(12)" : "+v"(tk));                                                \
    if (tid == 0) su[L_TK + par] = tk;
#define POOL_NEXT()                                                                                \
    pc = f32x4{__builtin_amdgcn_exp2f((bflo(sn[0]) - Mh) * L2E), __builtin_amdgcn_exp2f((bfhi(sn[0]) - Mh) * L2E), \
               __builtin_amdgcn_exp2f((bflo(sn[1]) - Mh) * L2E), __builtin_amdgcn_exp2f((bfhi(sn[1]) - Mh) * L2E)}; \
    tc = tn;                                                                                      \
    tn = __builtin_amdgcn_readfirstlane(su[L_TK + par]);                                           \
    par ^= 1;
  {
    const f32x4 pv_ = *(LAS f32x4*)(sc + 11 * RD);
    POOL_STEP(pv_);
    PUBLISH(0, vpub, SLOT(x + 16, m));
    PUT(part0, vpub, SLOT(x, m));
    PUT(part1, vpub, SLOT(x + 8, m));
    POOL_NEXT();
  }
  _Pragma("nounroll") while (tn < NTASK) {
    ACC_ZERO();
    POOL_STEP(pc);
    PUBLISH(par ^ 1, vpub, (24 + pool) * NTASK + tc);
    POOL_NEXT();
  }
  ACC_ZERO();
#define LAST_CHUNK(EXTRA)                                                                          \
    V_ITER(pc, false, 0u, 0u, EXTRA);                                                              \
    PUBLISH(par ^ 1, tc < NTASK ? vpub : OOB, (24 + pool) * NTASK + (tc < NTASK ? tc : 0));

  if (!is_mlp) {
    const auto ro = __builtin_amdgcn_make_buffer_rsrc((void*)p.wo, 0, HD * D * 2, 0x00020000);
    u32x4 w[6], ml, v0[4];
    const unsigned vwo = (unsigned)((tid >> 4) * (D * 2) + (tid & 15) * 16);
    const unsigned vs = (unsigned)(lane < 48 ? lane : 47) * 16, ss = (WS_S + SLOT(hd, wave) * DH) * 4, sst = (SLOT(hd, 1) - SLOT(hd, 0)) * (DH * 4);
    LAST_CHUNK(
      if (step < 6) w[step] = LD128(ro, vwo, (hd * DH + step * 32) * (D * 2) + sl * 256, 0);
      if (step == 5) ml = LD128(rw, (unsigned)(lane & 15) * 16, (WS_ML + hd * 64) * 4, SC1);
      if (step >= 8) v0[step - 8] = LD128(rw, vs, ss + 8 * (step - 8) * sst, SC1));
    UNROLL for (int i = 0; i < 6; ++i) *(LAS u32x4*)(su + L_WO + (i * NTH + tid) * 4) = w[i];
    float wt[4];
    {
      float M = fmaxf(u2f(ml[0]), u2f(ml[2]));
      M = fmaxf(M, dpp<XOR1>(M)); M = fmaxf(M, dpp<XOR2>(M)); M = fmaxf(M, dpp<XOR7>(M)); M = fmaxf(M, dpp<ROR8>(M));
      const float e0 = __builtin_amdgcn_exp2f((u2f(ml[0]) - M) * L2E), e1 = __builtin_amdgcn_exp2f((u2f(ml[2]) - M) * L2E);
      float L = e0 * u2f(ml[1]) + e1 * u2f(ml[3]);
      L += dpp<XOR1>(L); L += dpp<XOR2>(L); L += dpp<XOR7>(L); L += dpp<ROR8>(L);
      const float ew = (late ? 1.f : (wave & 1) ? e1 : e0) / L;
      UNROLL for (int j = 0; j < 4; ++j) wt[j] = lane_val(ew, (wave >> 1) + 4 * j);
    }
    lds_barrier();
    bf16x8 wof[6];
    UNROLL for (int t = 0; t < 6; ++t) wof[t] = frag_tr<256>((LAS unsigned char*)(su + L_WO) + t * 32 * 256 + wave * 32, lane);
    unsigned vyb = (lane & 15) == 0 ? lane : OOB;
    LAS float* sdst = sm + L_RED + par * (NW * DH) + wave * DH + (lane < 48 ? lane : 47) * 4;
    LAS float* ssrc = redr + par * (NW * DH);
    const int hcol = (lane & 3) * 4 + ((lane >> 4) & 1) + 2 * (lane >> 5);
    const unsigned xw = su[L_X + (late ? x * 5 + rho : 0) * 8 + (hcol >> 1)];
    float xcol = (hcol & 1) ? bfhi(xw) : bflo(xw);
    unsigned tagc = (lane & 32) ? ((lane & 16) ? tag4[3] : tag4[2]) : ((lane & 16) ? tag4[1] : tag4[0]);
    unsigned vh = (unsigned)((lane >> 2) * D + (lane & 3) * 4) * 4, vhs = lane < 4 ? (unsigned)lane * 16 : OOB;
    PIN(vyb); PIN(sdst); PIN(ssrc); PIN(xcol); PIN(tagc); PIN(vh); PIN(vhs);
    f32x4 S[4];
    UNROLL for (int q = 0; q < 4; ++q) S[q] = f32x4{0.f, 0.f, 0.f, 0.f};
    unsigned need = late ? 15 : 1;
    UNROLL for (int j = 0; j < 4; ++j) v0[j] ^= tag4;
    if (fresh((or4(v0[0]) | or4(v0[1])) | (or4(v0[2]) | or4(v0[3])))) {
      S[0] = wsum4(v0, wt);
      need &= ~1u;
    }
    if (need) SPIN {
      u32x4 v[4][4];
      UNROLL for (int q = 0; q < 4; ++q)
        if (need & (1u << q)) { UNROLL for (int j = 0; j < 4; ++j) v[q][j] = LD128(rw, vs, ss + (32 * q + 8 * j) * sst, SC1); }
      UNROLL for (int q = 0; q < 4; ++q)
        if (need & (1u << q)) {
          UNROLL for (int j = 0; j < 4; ++j) v[q][j] ^= tag4;
          if (fresh((or4(v[q][0]) | or4(v[q][1])) | (or4(v[q][2]) | or4(v[q][3])))) {
            S[q] = wsum4(v[q], wt);
            need &= ~(1u << q);
          }
        }
      if (!need) break;
      REPOLL();
    }
    *(LAS f32x4*)sdst = (S[0] + S[1]) + (S[2] + S[3]);
    lds_barrier();
    if (wave < 3) {
      float t[NW];
      UNROLL for (int k = 0; k < NW; ++k) t[k] = ssrc[k * DH];
      ((LAS __bf16*)(su + L_O))[tid] = (__bf16)(((t[0] + t[1]) + (t[2] + t[3])) + ((t[4] + t[5]) + (t[6] + t[7])));
    }
    lds_barrier();
    bf16x8 of[6];
    UNROLL for (int t = 0; t < 6; ++t) of[t] = frag_vec(su + L_O, lane, t);
    SB();
    f32x4 y0 = {0.f, 0.f, 0.f, 0.f}, y1 = {0.f, 0.f, 0.f, 0.f};
    UNROLL for (int t = 0; t < 6; t += 2) {
      y0 = __builtin_amdgcn_mfma_f32_16x16x32_bf16(wof[t], of[t], y0, 0, 0, 0);
      y1 = __builtin_amdgcn_mfma_f32_16x16x32_bf16(wof[t + 1], of[t + 1], y1, 0, 0, 0);
    }
    y0 += y1;
    __builtin_amdgcn_raw_buffer_store_b128(u32x4{mark(y0[0], tag4[0]), mark(y0[1], tag4[1]), mark(y0[2], tag4[2]), mark(y0[3], tag4[3])}, rw, vyb, (WS_B + hd * D + sl * 128 + wave * 16) * 4, SC1);
    if (late && wave == 3) {
      const int fj = x * 5 + rho;
      u32x4 v[2];
      SPIN {
        UNROLL for (int i = 0; i < 2; ++i) v[i] = LD128(rw, vh, (WS_B + i * 16 * D + fj * 16) * 4, SC1);
        UNROLL for (int i = 0; i < 2; ++i) v[i] ^= tag4;
        if (fresh(or4(v[0]) | or4(v[1]))) break;
        REPOLL();
      }
      const f32x4 t = as_f4(v[0]) + as_f4(v[1]);
      float e = tr16(tr32(t[0], t[2]), tr32(t[1], t[3]));
      e += dpp<ROR8>(e); e += dpp<ROR4>(e);
      const unsigned hw = f2u(rb(xcol + rb(e))) | tagc;
      const auto h16 = __builtin_amdgcn_permlane16_swap(hw, hw, false, false);
      const auto h02 = __builtin_amdgcn_permlane32_swap(h16[0], h16[0], false, false), h13 = __builtin_amdgcn_permlane32_swap(h16[1], h16[1], false, false);
      __builtin_amdgcn_raw_buffer_store_b128(u32x4{h02[0], h13[0], h02[1], h13[1]}, rw, vhs, (WS_H + fj * 16) * 4, SC1);
    }
    __builtin_amdgcn_endpgm();
  }

  LAST_CHUNK( );
  const auto rgu = __builtin_amdgcn_make_buffer_rsrc((void*)(wave < 4 ? p.wg : p.wu), 0, D * F * 2, 0x00020000);
  const auto rd_ = __builtin_amdgcn_make_buffer_rsrc((void*)p.wd, 0, D * F * 2, 0x00020000);
  const int t8 = tid & 255;
  u32x4 w[8];
#define MLP_LOADS()                                                                                \
  UNROLL for (int i = 0; i < 3; ++i) { w[5 + i] = LD128(rd_, i < 2 || tid < 256 ? (unsigned)(i * NTH + tid) * 16 : OOB, mj * (FPW * D * 2), 0); SB(); } \
  UNROLL for (int i = 0; i < 5; ++i) { w[i] = LD128(rgu, (unsigned)((t8 >> 1) * (F * 2) + (t8 & 1) * 16), i * 128 * (F * 2) + mj * (FPW * 2), 0); SB(); }
#define STAGE_D() UNROLL for (int i = 0; i < 3; ++i) if (i < 2 || tid < 256) *(LAS u32x4*)(su + L_WD + (i * NTH + tid) * 4) = w[5 + i]; lds_barrier();
#define STAGE_GU() UNROLL for (int i = 0; i < 5; ++i) *(LAS u32x4*)(su + L_WG + (wave >> 2) * (D * 8) + (i * 256 + t8) * 4) = w[i]; lds_barrier();
  if (wave < 3) {
    MLP_LOADS();
    STAGE_D();
    STAGE_GU();
    __builtin_amdgcn_s_barrier();
    __builtin_amdgcn_endpgm();
  }
  const int g = wave - 3;
  MLP_LOADS();
  STAGE_D();
  unsigned wd0[8], wd1[8], hpair;
  UNROLL for (int i = 0; i < 8; ++i) {
    const unsigned wa = su[L_WD + 2 * i * (D / 2) + g * 64 + lane], wb = su[L_WD + (2 * i + 1) * (D / 2) + g * 64 + lane];
    wd0[i] = __builtin_amdgcn_perm(wb, wa, 0x05040100u); wd1[i] = __builtin_amdgcn_perm(wb, wa, 0x07060302u);
    PIN(wd0[i]); PIN(wd1[i]);
  }
  LAS float* gsrc = sm + (lane & 15);
  LAS unsigned* hslot = su + g * 64 + lane;
  LAS unsigned* hfrag = su + L_H + g * 64 + 4 * (lane >> 4);
  LAS float* gdst = sm + L_GU + g * 32 + (lane & 31);
  LAS f32x2* sdst = (LAS f32x2*)(sm + L_XN + 160 + 2 * g);
  unsigned tagd0 = (lane & 1) ? tag4[2] : tag4[0], tagd1 = (lane & 1) ? tag4[3] : tag4[1];
  unsigned vdst = (lane & 1) ? OOB : (unsigned)lane * 8;
  PIN(tagd0); PIN(tagd1); PIN(vdst);
  PIN(hslot); PIN(hfrag); PIN(gdst); PIN(sdst); PIN(gsrc);
  STAGE_GU();
  bf16x8 wgf[4], wuf[4];
  UNROLL for (int t = 0; t < 4; ++t) {
    wgf[t] = frag_tr<32>((LAS unsigned char*)(su + L_WG) + (g * 128 + t * 32) * 32, lane);
    wuf[t] = frag_tr<32>((LAS unsigned char*)(su + L_WG + D * 8) + (g * 128 + t * 32) * 32, lane);
  }
  {
    const bf16x8 ones = __builtin_bit_cast(bf16x8, u32x4{0x3F803F80u, 0x3F803F80u, 0x3F803F80u, 0x3F803F80u});
    f32x4 cg = {0.f, 0.f, 0.f, 0.f}, cu = {0.f, 0.f, 0.f, 0.f};
    UNROLL for (int t = 0; t < 4; ++t) {
      cg = __builtin_amdgcn_mfma_f32_16x16x32_bf16(ones, wgf[t], cg, 0, 0, 0);
      cu = __builtin_amdgcn_mfma_f32_16x16x32_bf16(ones, wuf[t], cu, 0, 0, 0);
    }
    gdst[L_XN - L_GU] = (lane & 16) ? cu[0] : cg[0];
  }
  {
    u32x2 hv;
    SPIN {
      hv = __builtin_amdgcn_raw_buffer_load_b64(rw, (unsigned)lane * 8, (WS_H + g * 128) * 4, SC1);
      if (fresh((hv[0] ^ tagd0) | (hv[1] ^ tagd1))) break;
      REPOLL();
    }
    hpair = __builtin_amdgcn_perm(hv[1], hv[0], 0x07060302u);
    hslot[L_H] = hpair;
    bf16x8 hf[4];
    UNROLL for (int t = 0; t < 4; ++t) hf[t] = __builtin_bit_cast(bf16x8, *(LAS u32x4*)(hfrag + 16 * t));
    const float h0 = bflo(hpair), h1 = bfhi(hpair), piv = lane_val(h0, 0), e0 = h0 - piv, e1 = h1 - piv;
    float s1 = e0 + e1, s2 = fmaf(e0, e0, e1 * e1);
    SB();
    f32x4 ga = {0.f, 0.f, 0.f, 0.f}, ua = {0.f, 0.f, 0.f, 0.f};
    UNROLL for (int t = 0; t < 4; ++t) {
      ga = __builtin_amdgcn_mfma_f32_16x16x32_bf16(hf[t], wgf[t], ga, 0, 0, 0);
      ua = __builtin_amdgcn_mfma_f32_16x16x32_bf16(hf[t], wuf[t], ua, 0, 0, 0);
    }
    float s12 = tr32(s1, s2);
    s12 += dpp<XOR1>(s12); s12 += dpp<XOR2>(s12); s12 += dpp<XOR7>(s12); s12 += dpp<ROR8>(s12);
    s12 += u2f(__builtin_amdgcn_update_dpp(0, f2u(s12), 0x142, 0xa, 0xf, false));
    s1 = lane_val(s12, 31); s2 = lane_val(s12, 63);
    *sdst = f32x2{fmaf(s1, 1.f / 128, piv), fmaf(s1 * s1, -1.f / 128, s2)};
    *gdst = (lane & 16) ? ua[0] : ga[0];
  }
  lds_barrier();
  {
    const f32x4 sa = *(LAS f32x4*)(sm + L_XN + 160), sb = *(LAS f32x4*)(sm + L_XN + 164);
    const f32x2 sc2 = *(LAS f32x2*)(sm + L_XN + 168);
    float gs[5], us[5], cg[5], cu[5];
    UNROLL for (int w = 0; w < 5; ++w) { gs[w] = gsrc[L_GU + w * 32]; us[w] = gsrc[L_GU + w * 32 + 16]; cg[w] = gsrc[L_XN + w * 32]; cu[w] = gsrc[L_XN + w * 32 + 16]; }
    const float csg = ((cg[0] + cg[1]) + (cg[2] + cg[3])) + cg[4], csu = ((cu[0] + cu[1]) + (cu[2] + cu[3])) + cu[4];
    const float mean = (((sa[0] + sa[2]) + (sb[0] + sb[2])) + sc2[0]) * 0.2f;
    const float a0 = sa[0] - mean, a1 = sa[2] - mean, a2_ = sb[0] - mean, a3 = sb[2] - mean, a4 = sc2[0] - mean;
    const float m2 = fmaf(fmaf(a0, a0, a1 * a1) + fmaf(a2_, a2_, fmaf(a3, a3, a4 * a4)), 128.f, ((sa[1] + sa[3]) + (sb[1] + sb[3])) + sc2[1]);
    const float mu = rb(mean), rinv = __builtin_amdgcn_rcpf(rb(__builtin_amdgcn_sqrtf(rb(rb(m2 * (1.f / D)) + 1e-5f))));
    const float gt = rb(fmaf(-mu, csg, ((gs[0] + gs[1]) + (gs[2] + gs[3])) + gs[4]) * rinv);
    const float ut = rb(fmaf(-mu, csu, ((us[0] + us[1]) + (us[2] + us[3])) + us[4]) * rinv);
    const float act = rb(__builtin_amdgcn_rcpf(1.f + __builtin_amdgcn_exp2f(-L2E * gt))) * ut;
    const unsigned ap = pack_bf(act, dpp<XOR1>(act));
    unsigned a2[8];
    UNROLL for (int i = 0; i < 8; ++i) a2[i] = __builtin_amdgcn_readlane(ap, 2 * i);
    SB();
    float d0 = 0.f, d1 = 0.f;
    UNROLL for (int i = 0; i < 8; ++i) { d0 = dot2(wd0[i], a2[i], d0); d1 = dot2(wd1[i], a2[i], d1); }
    const unsigned e0 = mark(d0, tagd0), e1 = mark(d1, tagd1);
    __builtin_amdgcn_raw_buffer_store_b128(u32x4{e0, e1, f2u(dpp<XOR1>(u2f(e0))), f2u(dpp<XOR1>(u2f(e1)))}, rw, vdst, (WS_D + mj * D + g * 128) * 4, SC1);
  }
  if (mj >= NFIN || wave != 3) return;
  {
    const int col = (lane & 3) * 4 + ((lane >> 4) & 1) + 2 * (lane >> 5);
    const unsigned hw = su[L_H + mj * 8 + (col >> 1)];
    float hv = (col & 1) ? bfhi(hw) : bflo(hw);
    unsigned short* op = p.out + mj * 16 + col;
    PIN(hv); PIN(op);
    const unsigned vd = (unsigned)((lane >> 2) * D + (lane & 3) * 4) * 4;
    u32x4 v[6];
    SPIN {
      UNROLL for (int i = 0; i < 6; ++i) v[i] = LD128(rw, vd, (WS_D + i * 16 * D + mj * 16) * 4, SC1);
      UNROLL for (int i = 0; i < 6; ++i) v[i] ^= tag4;
      if (fresh(((or4(v[0]) | or4(v[1])) | (or4(v[2]) | or4(v[3]))) | (or4(v[4]) | or4(v[5])))) break;
      REPOLL();
    }
    const f32x4 t = ((as_f4(v[0]) + as_f4(v[1])) + (as_f4(v[2]) + as_f4(v[3]))) + (as_f4(v[4]) + as_f4(v[5]));
    float e = tr16(tr32(t[0], t[2]), tr32(t[1], t[3]));
    e += dpp<ROR8>(e); e += dpp<ROR4>(e);
    const __bf16 o = (__bf16)(hv + rb(e));
    if ((lane & 12) == 0) *(GAS unsigned short*)op = __builtin_bit_cast(unsigned short, o);
    if (mj == 0) __builtin_amdgcn_raw_buffer_store_b64(u32x2{tag_lo, tag_hi}, rw, lane == 0 ? 0 : OOB, WS_E * 4, SC1);
  }
}

}

extern "C" void kernel_launch(void* const* d_in, const int* in_sizes, int n_in, void* d_out, int out_size, void* d_ws, size_t ws_size,
                              hipStream_t stream) {
  Params p;
  p.x = (const unsigned short*)d_in[0];
  p.kc = (const unsigned short*)d_in[1];
  p.vc = (const unsigned short*)d_in[2];
  p.wq = (const unsigned short*)d_in[3];
  p.wo = (const unsigned short*)d_in[4];
  p.wg = (const unsigned short*)d_in[6];
  p.wu = (const unsigned short*)d_in[7];
  p.wd = (const unsigned short*)d_in[8];
  p.out = (unsigned short*)d_out;
  p.ws = (unsigned*)d_ws;
  (void)hipFuncSetAttribute((const void*)fused_decode, hipFuncAttributeMaxDynamicSharedMemorySize, LDS_BYTES);
  fused_decode<<<NWG, NTH, LDS_BYTES, stream>>>(p);
}
```
